```python
import torch

def _softmax(x, axis=-1):
    return torch.softmax(x, dim=axis)
def _gelu(x):
    return torch.nn.functional.gelu(x, approximate="tanh")
def _silu(x):
    return torch.nn.functional.silu(x)
def _sigmoid(x):
    return torch.sigmoid(x)
def _layernorm(x, axis=-1, eps=1e-5):
    m = x.mean(dim=axis, keepdim=True)
    v = x.var(dim=axis, keepdim=True, unbiased=False)
    return (x - m) / torch.sqrt(v + eps)
def _scatter_add(tgt, idx, src, axis=0):
    return tgt.clone().index_add_(axis, idx, src)
def _segment_sum(src, seg, n):
    out = torch.zeros((n,) + src.shape[1:], dtype=src.dtype)
    return out.index_add_(0, seg, src)
def _pad(x, pw):
    flat = tuple(v for pair in reversed(pw) for v in pair)
    return torch.nn.functional.pad(x, flat)

kernel_name = 'concat_heads__sliding_block_cached__mha_block_cached__decode'

def setup_inputs():

    _w = (lambda a: (lambda t: t.to(torch.bfloat16) if t.is_floating_point() else t)(torch.as_tensor(a)))
    def _A_setup():
        B, Sq, D = 1, 1, 512
        Hq, Hkv, Sk, Dh = 32, 8, 16384, 128
        assert Hq % Hkv == 0, (Hq, Hkv)
        return {
            "x": _w((lambda *s: torch.randn(*s, dtype=torch.bfloat16) * 0.8)(B, Sq, D)),
            "k_cache": _w((lambda *s: torch.randn(*s, dtype=torch.bfloat16) * 1.6)(B, Hkv, Sk, Dh)),
            "v_cache": _w((lambda *s: torch.randn(*s, dtype=torch.bfloat16) * 0.2)(B, Hkv, Sk, Dh)),
            "wq": _w((lambda *s: torch.randn(*s, dtype=torch.bfloat16) * 1.6)(D, Hq * Dh) * (D ** -0.5)),
            "wo": _w((lambda *s: torch.randn(*s, dtype=torch.bfloat16) * 0.2)(Hq * Dh, D) * ((Hq * Dh) ** -0.5)),
        }
    _A_in = _A_setup()

    def _B_setup():
        B, Sq, D = 1, 1, 512
        H, Sk, Dh = 32, 16384, 128
        return {
            "x": _w((lambda *s: torch.randn(*s, dtype=torch.bfloat16) * 0.8)(B, Sq, D)),
            "k_cache": _w((lambda *s: torch.randn(*s, dtype=torch.bfloat16) * 1.6)(B, H, Sk, Dh)),
            "v_cache": _w((lambda *s: torch.randn(*s, dtype=torch.bfloat16) * 0.2)(B, H, Sk, Dh)),
            "wq": _w((lambda *s: torch.randn(*s, dtype=torch.bfloat16) * 1.6)(D, H * Dh) * (D ** -0.5)),
            "wo": _w((lambda *s: torch.randn(*s, dtype=torch.bfloat16) * 0.2)(H * Dh, D) * ((H * Dh) ** -0.5)),
        }
    _B_in = _B_setup()

    return {
        "A_x": _A_in["x"],
        "A_k_cache": _A_in["k_cache"],
        "A_v_cache": _A_in["v_cache"],
        "A_wq": _A_in["wq"],
        "A_wo": _A_in["wo"],
        "B_x": _B_in["x"],
        "B_k_cache": _B_in["k_cache"],
        "B_v_cache": _B_in["v_cache"],
        "B_wq": _B_in["wq"],
        "B_wo": _B_in["wo"],
    }

def reference(A_x, A_k_cache, A_v_cache, A_wq, A_wo, B_x, B_k_cache, B_v_cache, B_wq, B_wo):
    def _A_ref(x, k_cache, v_cache, wq, wo):
        B, Sq, D = x.shape
        Hq, Dh = 32, 128
        Hkv, Sk = k_cache.shape[1], k_cache.shape[2]
        W = min(4096, max(256, Sk // 4))
        rep = Hq // Hkv
        q = (lambda x, *d: x.permute(*d))((x @ wq).reshape(B, Sq, Hq, Dh), 0, 2, 1, 3)
        k = (lambda x, n, axis: torch.repeat_interleave(x, n, dim=axis))(k_cache[:, :, Sk - W:, :], rep, axis=1)
        v = (lambda x, n, axis: torch.repeat_interleave(x, n, dim=axis))(v_cache[:, :, Sk - W:, :], rep, axis=1)
        scores = torch.einsum("bhqd,bhkd->bhqk", q, k) * (Dh ** -0.5)
        p = _softmax(scores, axis=-1)
        o = torch.einsum("bhqk,bhkd->bhqd", p, v)
        return (lambda x, *d: x.permute(*d))(o, 0, 2, 1, 3).reshape(B, Sq, Hq * Dh) @ wo

    def _B_ref(x, k_cache, v_cache, wq, wo):
        B, Sq, D = x.shape
        H, Dh = 32, 128
        q = (lambda x, *d: x.permute(*d))((x @ wq).reshape(B, Sq, H, Dh), 0, 2, 1, 3)
        scores = torch.einsum("bhqd,bhkd->bhqk", q, k_cache) * (Dh ** -0.5)
        p = _softmax(scores, axis=-1)
        o = torch.einsum("bhqk,bhkd->bhqd", p, v_cache)
        return (lambda x, *d: x.permute(*d))(o, 0, 2, 1, 3).reshape(B, Sq, H * Dh) @ wo

    a = _A_ref(A_x, A_k_cache, A_v_cache, A_wq, A_wo)
    b = _B_ref(B_x, B_k_cache, B_v_cache, B_wq, B_wo)
    return torch.concatenate([a, b], axis=-1)
```

```cpp
#include <hip/hip_runtime.h>
#include <hip/hip_bf16.h>
#include <stdint.h>

typedef unsigned u32;
typedef unsigned long long u64;
typedef u32 u32x4 __attribute__((ext_vector_type(4)));
typedef u32 u32x2 __attribute__((ext_vector_type(2)));
typedef float f32x4 __attribute__((ext_vector_type(4)));
typedef float f32x2 __attribute__((ext_vector_type(2)));
typedef __bf16 bf16x2 __attribute__((ext_vector_type(2)));
#define GAS __attribute__((address_space(1)))
#define LAS __attribute__((address_space(3)))
#define RLX_AGENT __ATOMIC_RELAXED, __HIP_MEMORY_SCOPE_AGENT
#define RLX_WG __ATOMIC_RELAXED, __HIP_MEMORY_SCOPE_WORKGROUP

namespace {

constexpr int DM = 512, NH = 32, DH = 128, SK = 16384, WIN = 4096, HKV = 8;
constexpr int NWG = 256, NSW = 8, NTHR = 640;
constexpr int QCOLS = NH * DH;
constexpr u32 NUNIT = SK / 32;
constexpr u32 UNIT_BYTES = 32 * DH * 2;
constexpr u32 NSTATIC = 128;
constexpr float C2 = 0.088388347648318440f * 1.4426950408889634f;
constexpr u32 SPIN_CAP = 1u << 18;

constexpr size_t WS_EPOCH = 0;
constexpr size_t WS_CNT = 4096;
constexpr size_t WS_QG = 16384;
constexpr size_t WS_PO = 65536;
constexpr size_t WS_PML = WS_PO + (size_t)2 * 32 * 8 * 128 * 8;
constexpr size_t WS_OP = WS_PML + (size_t)2 * 32 * 8 * 16;
constexpr size_t WS_END = WS_OP + (size_t)2 * 32 * 512 * 8;

struct Params {
  const uint16_t* x[2];
  const uint16_t* k[2];
  const uint16_t* v[2];
  const uint16_t* wq[2];
  const uint16_t* wo[2];
  uint16_t* out;
  unsigned char* ws;
};

constexpr int PREC = 132;
constexpr int L_QRED = 0;
constexpr int L_QB = L_QRED + 8 * 32;
constexpr int L_QA = L_QB + 64;
constexpr int L_OB = L_QA + 256;
constexpr int L_PB = L_OB + 128;
constexpr int L_PA = L_PB + 8 * PREC;
constexpr int L_FLAG = L_PA + 32 * PREC;
constexpr int L_WORDS = L_FLAG + 8;
enum { F_CNTQ = 0, F_QB = 1, F_QA = 2, F_CNTA = 3, F_CNTB = 4, F_TAGOK = 5, F_TAG = 6 };

__device__ __forceinline__ float dot2(u32 a, u32 b, float acc) {
  return __builtin_amdgcn_fdot2_f32_bf16(__builtin_bit_cast(bf16x2, a), __builtin_bit_cast(bf16x2, b), acc, false);
}
template <int CTRL> __device__ __forceinline__ float dpp(float x) {
  return __builtin_bit_cast(float, __builtin_amdgcn_mov_dpp(__builtin_bit_cast(int, x), CTRL, 0xf, 0xf, true));
}
constexpr int XOR1 = 0xB1, XOR2 = 0x4E, XOR7 = 0x141, XOR15 = 0x140, XOR8 = 0x128, ROR4 = 0x124, BCAST = 0x150;
__device__ __forceinline__ float xrow_max(float x) {
  auto s = __builtin_amdgcn_permlane16_swap(__float_as_uint(x), __float_as_uint(x), false, false);
  x = fmaxf(__uint_as_float(s[0]), __uint_as_float(s[1]));
  auto t = __builtin_amdgcn_permlane32_swap(__float_as_uint(x), __float_as_uint(x), false, false);
  return fmaxf(__uint_as_float(t[0]), __uint_as_float(t[1]));
}
__device__ __forceinline__ float xrow_sum(float x) {
  auto s = __builtin_amdgcn_permlane16_swap(__float_as_uint(x), __float_as_uint(x), false, false);
  x = __uint_as_float(s[0]) + __uint_as_float(s[1]);
  auto t = __builtin_amdgcn_permlane32_swap(__float_as_uint(x), __float_as_uint(x), false, false);
  return __uint_as_float(t[0]) + __uint_as_float(t[1]);
}
__device__ __forceinline__ float bf_lo(u32 w) { return __uint_as_float(w << 16); }
__device__ __forceinline__ float bf_hi(u32 w) { return __uint_as_float(w & 0xffff0000u); }
__device__ __forceinline__ u32 pack_bf16(float lo, float hi) {
  const __hip_bfloat16 a = __float2bfloat16(lo), b = __float2bfloat16(hi);
  return (u32)__builtin_bit_cast(uint16_t, a) | ((u32)__builtin_bit_cast(uint16_t, b) << 16);
}
__device__ __forceinline__ float ex2(float x) { return __builtin_amdgcn_exp2f(x); }

__device__ __forceinline__ void lds_wait(LAS u32* w, u32 want) {
  while (__hip_atomic_load(w, RLX_WG) < want) __builtin_amdgcn_s_sleep(1);
  asm volatile("" ::: "memory");
}
__device__ __forceinline__ void lds_signal_add(LAS u32* w, int lane) {
  asm volatile("s_waitcnt lgkmcnt(0)" ::: "memory");
  if (lane == 0) __hip_atomic_fetch_add(w, 1u, RLX_WG);
}

#define STAMP(i) do { } while (0)

#define FENCE() do { asm volatile("" ::: "memory"); __builtin_amdgcn_sched_barrier(0); } while (0)
#define PIN1(a) asm volatile("" : "+v"(a) :: "memory")
#define PIN4(a, b, c, d) asm volatile("" : "+v"(a), "+v"(b), "+v"(c), "+v"(d) :: "memory")
#define PIN8(a) asm volatile("" : "+v"((a)[0]), "+v"((a)[1]), "+v"((a)[2]), "+v"((a)[3]), "+v"((a)[4]), "+v"((a)[5]), "+v"((a)[6]), "+v"((a)[7]) :: "memory")
typedef __amdgpu_buffer_rsrc_t rsrc_t;
__device__ __forceinline__ rsrc_t mk_rsrc(const void* base, u32 bytes) {
  return __builtin_amdgcn_make_buffer_rsrc(const_cast<void*>(base), 0, bytes, 0x00020000);
}
__device__ __forceinline__ u32x4 ld_nt(rsrc_t r, u32 voff, u32 soff) { return __builtin_amdgcn_raw_buffer_load_b128(r, voff, soff, 2); }
__device__ __forceinline__ u32x4 ld_sc1(rsrc_t r, u32 voff, u32 soff) { return __builtin_amdgcn_raw_buffer_load_b128(r, voff, soff, 16); }
__device__ __forceinline__ void st_sc1(u32x4 v, rsrc_t r, u32 voff, u32 soff) { __builtin_amdgcn_raw_buffer_store_b128(v, r, voff, soff, 16); }

__device__ __forceinline__ u32 claim(GAS u32* cnt, int lane) {
  u32 off = 0, v = 0;
  asm volatile("" : "+v"(off));
  if (lane == 0) v = __hip_atomic_fetch_add(cnt + off, 1u, RLX_AGENT);
  return v;
}

#define TR_STEP(HALF, CTRL, BIT) _Pragma("unroll") for (int h_ = 0; h_ < (HALF); ++h_) { \
    const float lo_ = x[h_], hi_ = x[h_ + (HALF)]; const float t1_ = lo_ + dpp<CTRL>(lo_), t2_ = hi_ + dpp<CTRL>(hi_); \
    x[h_] = (c & (BIT)) ? t2_ : t1_; }

__global__ void __launch_bounds__(NTHR) fused_decode_kernel(Params p) {
  __shared__ __attribute__((aligned(16))) u32 lds_raw[L_WORDS];
  LAS u32* lds = (LAS u32*)lds_raw;
  LAS float* ldsf = (LAS float*)lds_raw;
  const int tid = threadIdx.x, lane = tid & 63;
  const int wave = __builtin_amdgcn_readfirstlane(tid >> 6);
  const int b = blockIdx.x;
  const int h = b >> 3, s = b & 7;
  const bool isA = ((s & 3) == (h & 3));
  const int g = h >> 2;
  const int sA = (h & 3) * 2 + (s >> 2);
  const int cb = (b & 7) * 32 + (b >> 3);
  const int brq = cb >> 7, col0 = (cb & 127) * 32;

  if (wave < NSW) {
    const int r = lane >> 4, c = lane & 15;
    const u32 voff = (u32)lane * 16u;
    const int rg = tid >> 2, cq = tid & 3;
    const u32x2 xv = *(const GAS u32x2*)(p.x[brq] + 4 * rg);
    u32x4 wv[4];
    {
      const GAS u32x4* wp = (const GAS u32x4*)(p.wq[brq] + (size_t)(4 * rg) * QCOLS + col0 + 8 * cq);
#pragma unroll
      for (int i = 0; i < 4; ++i) wv[i] = __builtin_nontemporal_load(wp + (size_t)i * (QCOLS / 8));
    }
    FENCE();
    const rsrc_t rKB = mk_rsrc(p.k[1] + (size_t)h * SK * DH, (u32)SK * DH * 2);
    const rsrc_t rVB = mk_rsrc(p.v[1] + (size_t)h * SK * DH, (u32)SK * DH * 2);
    const rsrc_t rKA = mk_rsrc(p.k[0] + ((size_t)g * SK + (SK - WIN)) * DH, (u32)WIN * DH * 2);
    const rsrc_t rVA = mk_rsrc(p.v[0] + ((size_t)g * SK + (SK - WIN)) * DH, (u32)WIN * DH * 2);
    const u32 id0 = (u32)(s * 8 + wave), id1 = id0 + 64;
    const u32 aoff = (u32)(sA * 512 + wave * 64) * (DH * 2);
    u32x4 ring[16];
    if (isA) {
#pragma unroll
      for (int m2 = 0; m2 < 2; ++m2) {
#pragma unroll
        for (int i = 0; i < 4; ++i) ring[m2 * 8 + i] = ld_nt(rKA, voff + i * 1024, aoff + m2 * 4096);
#pragma unroll
        for (int i = 0; i < 4; ++i) ring[m2 * 8 + 4 + i] = ld_nt(rVA, voff + i * 1024, aoff + m2 * 4096);
      }
    } else {
#pragma unroll
      for (int i = 0; i < 8; ++i) ring[i] = ld_nt(rKB, voff + i * 1024, id0 * UNIT_BYTES);
#pragma unroll
      for (int i = 0; i < 8; ++i) ring[8 + i] = ld_nt(rVB, voff + i * 1024, id0 * UNIT_BYTES);
    }
    FENCE();
    if (tid < 8) lds[L_FLAG + tid] = 0u;
    asm volatile("s_waitcnt lgkmcnt(0)" ::: "memory");
    __builtin_amdgcn_s_barrier();
    if (wave == 0) STAMP(0);
    {
      float a[8];
      const float x0 = bf_lo(xv[0]), x1 = bf_hi(xv[0]), x2 = bf_lo(xv[1]), x3 = bf_hi(xv[1]);
#pragma unroll
      for (int k = 0; k < 4; ++k) {
        a[2 * k] = x0 * bf_lo(wv[0][k]) + x1 * bf_lo(wv[1][k]) + x2 * bf_lo(wv[2][k]) + x3 * bf_lo(wv[3][k]);
        a[2 * k + 1] = x0 * bf_hi(wv[0][k]) + x1 * bf_hi(wv[1][k]) + x2 * bf_hi(wv[2][k]) + x3 * bf_hi(wv[3][k]);
      }
#pragma unroll
      for (int k = 0; k < 8; ++k) { float t = a[k]; t += dpp<ROR4>(t); t += dpp<XOR8>(t); a[k] = xrow_sum(t); }
      if (lane < 4) {
        *(LAS f32x4*)(ldsf + L_QRED + wave * 32 + lane * 8) = f32x4{a[0], a[1], a[2], a[3]};
        *(LAS f32x4*)(ldsf + L_QRED + wave * 32 + lane * 8 + 4) = f32x4{a[4], a[5], a[6], a[7]};
      }
      lds_signal_add(lds + L_FLAG + F_CNTQ, lane);
    }
    if (wave == 0) STAMP(1);
    if (isA) {
      lds_wait(lds + L_FLAG + F_QA, 1u);
      u32x4 qa[4];
#pragma unroll
      for (int j = 0; j < 4; ++j) qa[j] = *(LAS u32x4*)(lds + L_QA + j * 64 + 4 * c);
      float mA = -INFINITY, lA = 0.f, acc[4][8];
#pragma unroll
      for (int j = 0; j < 4; ++j)
#pragma unroll
        for (int k = 0; k < 8; ++k) acc[j][k] = 0.f;
#pragma unroll
      for (int mt = 0; mt < 4; ++mt) {
        const int sl = (mt & 1) * 8;
        float x[16];
#pragma unroll
        for (int t = 0; t < 4; ++t) {
          const u32x4 kk = ring[sl + t];
#pragma unroll
          for (int j = 0; j < 4; ++j) {
            float d = dot2(kk[0], qa[j][0], 0.f); d = dot2(kk[1], qa[j][1], d); d = dot2(kk[2], qa[j][2], d); d = dot2(kk[3], qa[j][3], d);
            x[4 * j + t] = d;
          }
          PIN4(x[t], x[4 + t], x[8 + t], x[12 + t]);
          if (mt < 2) ring[sl + t] = ld_nt(rKA, voff + t * 1024, aoff + (mt + 2) * 4096);
          else ring[sl + t] = ld_nt(mt == 2 ? rKB : rVB, voff + t * 1024, id0 * UNIT_BYTES);
          FENCE();
        }
        TR_STEP(8, XOR8, 8) TR_STEP(4, XOR7, 4) TR_STEP(2, XOR2, 2) TR_STEP(1, XOR1, 1)
        const float sc = x[0];
        float pm = fmaxf(sc, dpp<XOR1>(sc)); pm = fmaxf(pm, dpp<XOR2>(pm));
        const float mn = fmaxf(mA, pm);
        const float al = ex2((mA - mn) * C2), pp = ex2((sc - mn) * C2);
        lA = lA * al + pp; mA = mn;
        if (mt > 0) {
#pragma unroll
          for (int j = 0; j < 4; ++j) {
            const float aj = j == 0 ? dpp<BCAST + 0>(al) : j == 1 ? dpp<BCAST + 4>(al) : j == 2 ? dpp<BCAST + 8>(al) : dpp<BCAST + 12>(al);
#pragma unroll
            for (int k = 0; k < 8; ++k) acc[j][k] *= aj;
          }
        }
#define A_PV(T) { const u32x4 vv = ring[sl + 4 + (T)]; float vf[8]; \
          _Pragma("unroll") for (int k = 0; k < 4; ++k) { vf[2 * k] = bf_lo(vv[k]); vf[2 * k + 1] = bf_hi(vv[k]); } \
          const float p0 = dpp<BCAST + 0 + (T)>(pp), p1 = dpp<BCAST + 4 + (T)>(pp), p2 = dpp<BCAST + 8 + (T)>(pp), p3 = dpp<BCAST + 12 + (T)>(pp); \
          _Pragma("unroll") for (int k = 0; k < 8; ++k) { acc[0][k] += p0 * vf[k]; acc[1][k] += p1 * vf[k]; acc[2][k] += p2 * vf[k]; acc[3][k] += p3 * vf[k]; } \
          PIN8(acc[0]); PIN8(acc[1]); PIN8(acc[2]); PIN8(acc[3]); \
          if (mt < 2) ring[sl + 4 + (T)] = ld_nt(rVA, voff + (T) * 1024, aoff + (mt + 2) * 4096); \
          else ring[sl + 4 + (T)] = ld_nt(mt == 2 ? rKB : rVB, voff + (4 + (T)) * 1024, id0 * UNIT_BYTES); \
          FENCE(); }
        A_PV(0) A_PV(1) A_PV(2) A_PV(3)
#undef A_PV
      }
      const float M = xrow_max(mA);
      const float f = ex2((mA - M) * C2);
      float L = lA * f; L += dpp<XOR1>(L); L += dpp<XOR2>(L); L = xrow_sum(L);
#pragma unroll
      for (int j = 0; j < 4; ++j) {
        const float fj = j == 0 ? dpp<BCAST + 0>(f) : j == 1 ? dpp<BCAST + 4>(f) : j == 2 ? dpp<BCAST + 8>(f) : dpp<BCAST + 12>(f);
#pragma unroll
        for (int k = 0; k < 8; ++k) acc[j][k] = xrow_sum(acc[j][k] * fj);
      }
      if (r == 0) {
#pragma unroll
        for (int j = 0; j < 4; ++j) {
          LAS float* rec = ldsf + L_PA + (wave * 4 + j) * PREC;
          *(LAS f32x4*)(rec + 8 * c) = f32x4{acc[j][0], acc[j][1], acc[j][2], acc[j][3]};
          *(LAS f32x4*)(rec + 8 * c + 4) = f32x4{acc[j][4], acc[j][5], acc[j][6], acc[j][7]};
        }
        if ((c & 3) == 0) { LAS float* rec = ldsf + L_PA + (wave * 4 + (c >> 2)) * PREC; rec[128] = M; rec[129] = L; }
      }
      lds_signal_add(lds + L_FLAG + F_CNTA, lane);
      if (wave == 0) STAMP(3);
    }
    lds_wait(lds + L_FLAG + F_QB, 1u);
    if (wave == 0) STAMP(2);
    const u32x4 q = *(LAS u32x4*)(lds + L_QB + 4 * c);
    GAS u32* cnt = (GAS u32*)(p.ws + WS_CNT + (size_t)h * 256);
    float m = -INFINITY, l = 0.f, acc[8];
#pragma unroll
    for (int k = 0; k < 8; ++k) acc[k] = 0.f;
    u32 unext = id1;
    u32 claimv = claim(cnt, lane);
    FENCE();
    for (;;) {
      const u32 soff = (unext < NUNIT ? unext : NUNIT) * UNIT_BYTES;
      float x[8];
#pragma unroll
      for (int i = 0; i < 8; ++i) {
        const u32x4 kk = ring[i];
        float d = dot2(kk[0], q[0], 0.f); d = dot2(kk[1], q[1], d); d = dot2(kk[2], q[2], d); d = dot2(kk[3], q[3], d);
        PIN1(d);
        x[i] = d;
        ring[i] = ld_nt(rKB, voff + i * 1024, soff);
        FENCE();
      }
      TR_STEP(4, XOR8, 8) TR_STEP(2, XOR7, 4) TR_STEP(1, XOR2, 2)
      const float sc = x[0] + dpp<XOR1>(x[0]);
      float pm = fmaxf(sc, dpp<XOR2>(sc)); pm = fmaxf(pm, dpp<XOR7>(pm)); pm = fmaxf(pm, dpp<XOR8>(pm));
      const float mn = fmaxf(m, pm);
      const float al = ex2((m - mn) * C2), pp = ex2((sc - mn) * C2);
      l = l * al + pp; m = mn;
#pragma unroll
      for (int k = 0; k < 8; ++k) acc[k] *= al;
#define B_PV(I) { const u32x4 vv = ring[8 + (I)]; const float pb = dpp<BCAST + 2 * (I)>(pp); \
        _Pragma("unroll") for (int k = 0; k < 4; ++k) { acc[2 * k] += pb * bf_lo(vv[k]); acc[2 * k + 1] += pb * bf_hi(vv[k]); } \
        PIN8(acc); \
        ring[8 + (I)] = ld_nt(rVB, voff + (I) * 1024, soff); FENCE(); }
      B_PV(0) B_PV(1) B_PV(2) B_PV(3) B_PV(4) B_PV(5) B_PV(6) B_PV(7)
#undef B_PV
      if (unext >= NUNIT) break;
      unext = (u32)__builtin_amdgcn_readfirstlane((int)claimv) + NSTATIC;
      claimv = claim(cnt, lane);
      FENCE();
    }
    if (wave == 0) STAMP(4);
    {
      const float M = xrow_max(m);
      const float f = ex2((m - M) * C2);
      float L = l * f; L += dpp<XOR1>(L); L += dpp<XOR2>(L); L += dpp<XOR7>(L); L += dpp<XOR8>(L); L = xrow_sum(L) * 0.5f;
#pragma unroll
      for (int k = 0; k < 8; ++k) acc[k] = xrow_sum(acc[k] * f);
      if (r == 0) {
        LAS float* rec = ldsf + L_PB + wave * PREC;
        *(LAS f32x4*)(rec + 8 * c) = f32x4{acc[0], acc[1], acc[2], acc[3]};
        *(LAS f32x4*)(rec + 8 * c + 4) = f32x4{acc[4], acc[5], acc[6], acc[7]};
        if (c == 0) { rec[128] = M; rec[129] = L; }
      }
      lds_signal_add(lds + L_FLAG + F_CNTB, lane);
    }
    return;
  }

  const int br = wave - NSW;
  const rsrc_t rWS = mk_rsrc(p.ws, (u32)WS_END);
  u32 tag = 0;
  if (br == 1) {
    const u32 e = __hip_atomic_load((GAS u32*)(p.ws + WS_EPOCH), RLX_AGENT);
    tag = e * 1664525u + 1013904223u;
  }
  __builtin_amdgcn_s_barrier();
  if (br == 1) {
    if (lane == 0) { lds[L_FLAG + F_TAG] = tag; }
    lds_signal_add(lds + L_FLAG + F_TAGOK, lane);
    lds_wait(lds + L_FLAG + F_CNTQ, (u32)NSW);
    float qs = 0.f;
    if (lane < 32) {
#pragma unroll
      for (int w = 0; w < NSW; ++w) qs += ldsf[L_QRED + w * 32 + lane];
    }
    const float qn = dpp<XOR1>(qs);
    const u32 pair = pack_bf16(qs, qn);
    if (brq == 1 && (col0 & 127) == 0) {
      if (lane == 0) __hip_atomic_store((GAS u32*)(p.ws + WS_CNT + (size_t)(col0 >> 7) * 256), 0u, RLX_AGENT);
      asm volatile("s_waitcnt vmcnt(0)" ::: "memory");
    }
    if (lane < 32 && (lane & 1) == 0) {
      GAS u64* dst = (GAS u64*)(p.ws + WS_QG) + (size_t)(brq * 32 + (col0 >> 7)) * 64 + ((col0 & 127) >> 1) + (lane >> 1);
      __hip_atomic_store(dst, ((u64)tag << 32) | pair, RLX_AGENT);
    }
    STAMP(8);
    GAS u64* src = (GAS u64*)(p.ws + WS_QG) + (size_t)(32 + h) * 64 + lane;
    u64 gq;
    for (u32 spins = 0;; ++spins) {
      gq = __hip_atomic_load(src, RLX_AGENT);
      if (__all((u32)(gq >> 32) == tag) || spins > SPIN_CAP) break;
      __builtin_amdgcn_s_sleep(2);
    }
    lds[L_QB + lane] = (u32)gq;
    lds_signal_add(lds + L_FLAG + F_QB, lane);
    STAMP(9);
  } else {
    lds_wait(lds + L_FLAG + F_TAGOK, 1u);
    tag = lds[L_FLAG + F_TAG];
    if (isA) {
      GAS u64* src = (GAS u64*)(p.ws + WS_QG) + (size_t)(4 * g) * 64 + lane;
      u64 gq[4];
      for (u32 spins = 0;; ++spins) {
        bool ok = true;
#pragma unroll
        for (int j = 0; j < 4; ++j) { gq[j] = __hip_atomic_load(src + j * 64, RLX_AGENT); ok &= (u32)(gq[j] >> 32) == tag; }
        if (__all(ok) || spins > SPIN_CAP) break;
        __builtin_amdgcn_s_sleep(2);
      }
#pragma unroll
      for (int j = 0; j < 4; ++j) lds[L_QA + j * 64 + lane] = (u32)gq[j];
      lds_signal_add(lds + L_FLAG + F_QA, lane);
    }
  }
  const int rq = lane >> 3, cg = lane & 7;
  u32 wp[8][8];
  {
    const GAS u32x4* w = (const GAS u32x4*)(p.wo[br] + ((size_t)h * DH + 2 * rq) * DM + s * 64 + cg * 8);
    u32x4 wa[8], wb[8];
#pragma unroll
    for (int i = 0; i < 8; ++i) {
      wa[i] = __builtin_nontemporal_load(w + (size_t)(16 * i) * (DM / 8));
      wb[i] = __builtin_nontemporal_load(w + (size_t)(16 * i + 1) * (DM / 8));
    }
#pragma unroll
    for (int i = 0; i < 8; ++i)
#pragma unroll
      for (int k = 0; k < 4; ++k) {
        wp[i][2 * k] = __builtin_amdgcn_perm(wb[i][k], wa[i][k], 0x05040100u);
        wp[i][2 * k + 1] = __builtin_amdgcn_perm(wb[i][k], wa[i][k], 0x07060302u);
      }
  }
  if (br == 1 || isA) {
    lds_wait(lds + L_FLAG + (br ? F_CNTB : F_CNTA), (u32)NSW);
    if (br == 1) STAMP(10);
    const int nh = br ? 1 : 4;
    for (int j = 0; j < nh; ++j) {
      const LAS float* rec = ldsf + (br ? L_PB : L_PA + j * PREC);
      const int stride = br ? PREC : 4 * PREC;
      float mw[NSW], M = -INFINITY;
#pragma unroll
      for (int w = 0; w < NSW; ++w) { mw[w] = rec[w * stride + 128]; M = fmaxf(M, mw[w]); }
      float L = 0.f, o0 = 0.f, o1 = 0.f;
#pragma unroll
      for (int w = 0; w < NSW; ++w) {
        const float f = ex2((mw[w] - M) * C2);
        const f32x2 o = *(const LAS f32x2*)(rec + w * stride + 2 * lane);
        L += f * rec[w * stride + 129]; o0 += f * o[0]; o1 += f * o[1];
      }
      const u32 slot = br ? (u32)((32 + h) * 8 + s) : (u32)((4 * g + j) * 8 + sA);
      st_sc1(u32x4{__float_as_uint(o0), tag, __float_as_uint(o1), tag}, rWS, (u32)WS_PO + slot * 1024u + (u32)lane * 16u, 0);
      if (lane == 0) st_sc1(u32x4{__float_as_uint(M * C2), tag, __float_as_uint(L), tag}, rWS, (u32)WS_PML + slot * 16u, 0);
    }
  }
  if (br == 1) STAMP(11);
  {
    const u32 slot0 = (u32)((br * 32 + h) * 8);
    u32x4 po[8], ml;
    for (u32 spins = 0;; ++spins) {
      asm volatile("" ::: "memory");
      ml = ld_sc1(rWS, (u32)WS_PML + (slot0 + (u32)(lane & 7)) * 16u, 0);
#pragma unroll
      for (int sp = 0; sp < 8; ++sp) po[sp] = ld_sc1(rWS, (u32)WS_PO + (slot0 + sp) * 1024u + (u32)lane * 16u, 0);
      bool ok = (ml[1] == tag) & (ml[3] == tag);
#pragma unroll
      for (int sp = 0; sp < 8; ++sp) ok &= (po[sp][1] == tag) & (po[sp][3] == tag);
      if (__all(ok) || spins > SPIN_CAP) break;
      if (br) __builtin_amdgcn_s_sleep(1); else __builtin_amdgcn_s_sleep(8);
    }
    if (br == 1) STAMP(12);
    const float ms = __uint_as_float(ml[0]);
    float M = fmaxf(ms, dpp<XOR1>(ms)); M = fmaxf(M, dpp<XOR2>(M)); M = fmaxf(M, dpp<XOR7>(M));
    const float f = ex2(ms - M);
    float L = f * __uint_as_float(ml[2]); L += dpp<XOR1>(L); L += dpp<XOR2>(L); L += dpp<XOR7>(L);
    float o0 = 0.f, o1 = 0.f;
#pragma unroll
    for (int sp = 0; sp < 8; ++sp) {
      const float fs = __builtin_bit_cast(float, __builtin_amdgcn_readlane(__builtin_bit_cast(int, f), sp));
      o0 += fs * __uint_as_float(po[sp][0]); o1 += fs * __uint_as_float(po[sp][2]);
    }
    const float inv = 1.0f / L;
    lds[L_OB + br * 64 + lane] = pack_bf16(o0 * inv, o1 * inv);
    asm volatile("s_waitcnt lgkmcnt(0)" ::: "memory");
    float y[8];
#pragma unroll
    for (int j = 0; j < 8; ++j) y[j] = 0.f;
#pragma unroll
    for (int i = 0; i < 8; ++i) {
      const u32 ob = lds[L_OB + br * 64 + rq + 8 * i];
#pragma unroll
      for (int j = 0; j < 8; ++j) y[j] = dot2(wp[i][j], ob, y[j]);
    }
#pragma unroll
    for (int j = 0; j < 4; ++j) {
      const float t1 = y[j] + dpp<XOR8>(y[j]), t2 = y[j + 4] + dpp<XOR8>(y[j + 4]);
      y[j] = (lane & 8) ? t2 : t1;
    }
#pragma unroll
    for (int j = 0; j < 2; ++j) {
      auto sw = __builtin_amdgcn_permlane16_swap(__float_as_uint(y[j]), __float_as_uint(y[j + 2]), false, false);
      y[j] = __uint_as_float(sw[0]) + __uint_as_float(sw[1]);
    }
    auto sw = __builtin_amdgcn_permlane32_swap(__float_as_uint(y[0]), __float_as_uint(y[1]), false, false);
    const float yy = __uint_as_float(sw[0]) + __uint_as_float(sw[1]);
    const int col = 8 * cg + ((lane >> 3) & 1) * 4 + ((lane >> 4) & 1) * 2 + (lane >> 5);
    GAS u64* dst = (GAS u64*)(p.ws + WS_OP) + (size_t)(br * 32 + h) * 512 + s * 64 + col;
    __hip_atomic_store(dst, ((u64)tag << 32) | __float_as_uint(yy), RLX_AGENT);
  }
  if (br == 1) STAMP(13);
  {
    GAS u64* src = (GAS u64*)(p.ws + WS_OP) + (size_t)(br * 32 + (lane >> 1)) * 512 + 2 * b + (lane & 1);
    u64 gy;
    for (u32 spins = 0;; ++spins) {
      gy = __hip_atomic_load(src, RLX_AGENT);
      if (__all((u32)(gy >> 32) == tag) || spins > SPIN_CAP) break;
      if (br) __builtin_amdgcn_s_sleep(1); else __builtin_amdgcn_s_sleep(8);
    }
    float y = __uint_as_float((u32)gy);
    y += dpp<XOR2>(y); y += dpp<ROR4>(y); y += dpp<XOR8>(y);
    y = xrow_sum(y);
    const float yn = dpp<XOR1>(y);
    if (lane == 0) *(GAS u32*)(p.out + br * 512 + 2 * b) = pack_bf16(y, yn);
    if (br == 1 && b == 0 && lane == 0) __hip_atomic_store((GAS u32*)(p.ws + WS_EPOCH), tag, RLX_AGENT);
  }
  if (br == 1) STAMP(14);
}

}

extern "C" void kernel_launch(void* const* d_in, const int* in_sizes, int n_in, void* d_out, int out_size, void* d_ws,
                              size_t ws_size, hipStream_t stream) {
  Params p;
  p.x[0] = (const uint16_t*)d_in[0]; p.k[0] = (const uint16_t*)d_in[1]; p.v[0] = (const uint16_t*)d_in[2];
  p.wq[0] = (const uint16_t*)d_in[3]; p.wo[0] = (const uint16_t*)d_in[4];
  p.x[1] = (const uint16_t*)d_in[5]; p.k[1] = (const uint16_t*)d_in[6]; p.v[1] = (const uint16_t*)d_in[7];
  p.wq[1] = (const uint16_t*)d_in[8]; p.wo[1] = (const uint16_t*)d_in[9];
  p.out = (uint16_t*)d_out; p.ws = (unsigned char*)d_ws;
  hipLaunchKernelGGL(fused_decode_kernel, dim3(NWG), dim3(NTHR), 0, stream, p);
}
```

```cpp
#include <hip/hip_runtime.h>
#include <hip/hip_bf16.h>
#include <stdint.h>

typedef unsigned u32;
typedef unsigned long long u64;
typedef u32 u32x4 __attribute__((ext_vector_type(4)));
typedef u32 u32x2 __attribute__((ext_vector_type(2)));
typedef float f32x4 __attribute__((ext_vector_type(4)));
typedef float f32x2 __attribute__((ext_vector_type(2)));
typedef __bf16 bf16x2 __attribute__((ext_vector_type(2)));
#define GAS __attribute__((address_space(1)))
#define LAS __attribute__((address_space(3)))
#define RLX_AGENT __ATOMIC_RELAXED, __HIP_MEMORY_SCOPE_AGENT
#define RLX_WG __ATOMIC_RELAXED, __HIP_MEMORY_SCOPE_WORKGROUP

namespace {

constexpr int DM = 512, NH = 32, DH = 128, SK = 16384, WIN = 4096, HKV = 8;
constexpr int NWG = 256, NSW = 8, NTHR = 640;
constexpr int QCOLS = NH * DH;
constexpr u32 NUNIT = SK / 32;
constexpr u32 UNIT_BYTES = 32 * DH * 2;
constexpr u32 NSTATIC = 128;
constexpr float C2 = 0.088388347648318440f * 1.4426950408889634f;
constexpr u32 SPIN_CAP = 1u << 18;

constexpr size_t WS_EPOCH = 0;
constexpr size_t WS_CNT = 4096;
constexpr size_t WS_QG = 16384;
constexpr size_t WS_PO = 65536;
constexpr size_t WS_PML = WS_PO + (size_t)2 * 32 * 8 * 128 * 8;
constexpr size_t WS_OP = WS_PML + (size_t)2 * 32 * 8 * 16;
constexpr size_t WS_END = WS_OP + (size_t)2 * 32 * 512 * 8;

struct Params {
  const uint16_t* x[2];
  const uint16_t* k[2];
  const uint16_t* v[2];
  const uint16_t* wq[2];
  const uint16_t* wo[2];
  uint16_t* out;
  unsigned char* ws;
};

constexpr int PREC = 132;
constexpr int L_QRED = 0;
constexpr int L_QB = L_QRED + 8 * 32;
constexpr int L_QA = L_QB + 64;
constexpr int L_OB = L_QA + 256;
constexpr int L_PB = L_OB + 128;
constexpr int L_PA = L_PB + 8 * PREC;
constexpr int L_FLAG = L_PA + 32 * PREC;
constexpr int L_WORDS = L_FLAG + 8;
enum { F_CNTQ = 0, F_QB = 1, F_QA = 2, F_CNTA = 3, F_CNTB = 4, F_TAGOK = 5, F_TAG = 6 };

__device__ __forceinline__ float dot2(u32 a, u32 b, float acc) {
  return __builtin_amdgcn_fdot2_f32_bf16(__builtin_bit_cast(bf16x2, a), __builtin_bit_cast(bf16x2, b), acc, false);
}
template <int CTRL> __device__ __forceinline__ float dpp(float x) {
  return __builtin_bit_cast(float, __builtin_amdgcn_mov_dpp(__builtin_bit_cast(int, x), CTRL, 0xf, 0xf, true));
}
constexpr int XOR1 = 0xB1, XOR2 = 0x4E, XOR7 = 0x141, XOR15 = 0x140, XOR8 = 0x128, ROR4 = 0x124, BCAST = 0x150;
__device__ __forceinline__ float xrow_max(float x) {
  auto s = __builtin_amdgcn_permlane16_swap(__float_as_uint(x), __float_as_uint(x), false, false);
  x = fmaxf(__uint_as_float(s[0]), __uint_as_float(s[1]));
  auto t = __builtin_amdgcn_permlane32_swap(__float_as_uint(x), __float_as_uint(x), false, false);
  return fmaxf(__uint_as_float(t[0]), __uint_as_float(t[1]));
}
__device__ __forceinline__ float xrow_sum(float x) {
  auto s = __builtin_amdgcn_permlane16_swap(__float_as_uint(x), __float_as_uint(x), false, false);
  x = __uint_as_float(s[0]) + __uint_as_float(s[1]);
  auto t = __builtin_amdgcn_permlane32_swap(__float_as_uint(x), __float_as_uint(x), false, false);
  return __uint_as_float(t[0]) + __uint_as_float(t[1]);
}
__device__ __forceinline__ float bf_lo(u32 w) { return __uint_as_float(w << 16); }
__device__ __forceinline__ float bf_hi(u32 w) { return __uint_as_float(w & 0xffff0000u); }
__device__ __forceinline__ u32 pack_bf16(float lo, float hi) {
  const __hip_bfloat16 a = __float2bfloat16(lo), b = __float2bfloat16(hi);
  return (u32)__builtin_bit_cast(uint16_t, a) | ((u32)__builtin_bit_cast(uint16_t, b) << 16);
}
__device__ __forceinline__ float ex2(float x) { return __builtin_amdgcn_exp2f(x); }

__device__ __forceinline__ void lds_wait(LAS u32* w, u32 want) {
  while (__hip_atomic_load(w, RLX_WG) < want) __builtin_amdgcn_s_sleep(1);
  asm volatile("" ::: "memory");
}
__device__ __forceinline__ void lds_signal_add(LAS u32* w, int lane) {
  asm volatile("s_waitcnt lgkmcnt(0)" ::: "memory");
  if (lane == 0) __hip_atomic_fetch_add(w, 1u, RLX_WG);
}

#define STAMP(i) do { } while (0)

#define FENCE() do { asm volatile("" ::: "memory"); __builtin_amdgcn_sched_barrier(0); } while (0)
#define PIN1(a) asm volatile("" : "+v"(a) :: "memory")
#define PIN4(a, b, c, d) asm volatile("" : "+v"(a), "+v"(b), "+v"(c), "+v"(d) :: "memory")
#define PIN8(a) asm volatile("" : "+v"((a)[0]), "+v"((a)[1]), "+v"((a)[2]), "+v"((a)[3]), "+v"((a)[4]), "+v"((a)[5]), "+v"((a)[6]), "+v"((a)[7]) :: "memory")
typedef __amdgpu_buffer_rsrc_t rsrc_t;
__device__ __forceinline__ rsrc_t mk_rsrc(const void* base, u32 bytes) {
  return __builtin_amdgcn_make_buffer_rsrc(const_cast<void*>(base), 0, bytes, 0x00020000);
}
#define AUX_K 2
#define AUX_V 2
#define AUX_A 2
template <int AUX> __device__ __forceinline__ u32x4 ld_x(rsrc_t r, u32 voff, u32 soff) { return __builtin_amdgcn_raw_buffer_load_b128(r, voff, soff, AUX); }
#define ld_kb ld_x<AUX_K>
#define ld_vb ld_x<AUX_V>
#define ld_a ld_x<AUX_A>
__device__ __forceinline__ u32x4 ld_sc1(rsrc_t r, u32 voff, u32 soff) { return __builtin_amdgcn_raw_buffer_load_b128(r, voff, soff, 16); }
__device__ __forceinline__ void st_sc1(u32x4 v, rsrc_t r, u32 voff, u32 soff) { __builtin_amdgcn_raw_buffer_store_b128(v, r, voff, soff, 16); }

__device__ __forceinline__ u32 claim(GAS u32* cnt, int lane) {
  u32 off = 0, v = 0;
  asm volatile("" : "+v"(off));
  if (lane == 0) v = __hip_atomic_fetch_add(cnt + off, 1u, RLX_AGENT);
  return v;
}

#define TR_STEP(HALF, CTRL, BIT) _Pragma("unroll") for (int h_ = 0; h_ < (HALF); ++h_) { \
    const float lo_ = x[h_], hi_ = x[h_ + (HALF)]; const float t1_ = lo_ + dpp<CTRL>(lo_), t2_ = hi_ + dpp<CTRL>(hi_); \
    x[h_] = (c & (BIT)) ? t2_ : t1_; }

template <int AK, int AV>
__device__ __forceinline__ void stream_waves(const Params& p, LAS u32* lds, LAS float* ldsf, const int tid, const int lane, const int wave, const int b, const int h, const int s,
                                             const bool isA, const int g, const int sA, const int brq, const int col0) {
    const int r = lane >> 4, c = lane & 15;
    const u32 voff = (u32)lane * 16u;
    const int rg = tid >> 2, cq = tid & 3;
    const u32x2 xv = *(const GAS u32x2*)(p.x[brq] + 4 * rg);
    u32x4 wv[4];
    {
      const GAS u32x4* wp = (const GAS u32x4*)(p.wq[brq] + (size_t)(4 * rg) * QCOLS + col0 + 8 * cq);
#pragma unroll
      for (int i = 0; i < 4; ++i) wv[i] = __builtin_nontemporal_load(wp + (size_t)i * (QCOLS / 8));
    }
    FENCE();
    const rsrc_t rKB = mk_rsrc(p.k[1] + (size_t)h * SK * DH, (u32)SK * DH * 2);
    const rsrc_t rVB = mk_rsrc(p.v[1] + (size_t)h * SK * DH, (u32)SK * DH * 2);
    const rsrc_t rKA = mk_rsrc(p.k[0] + ((size_t)g * SK + (SK - WIN)) * DH, (u32)WIN * DH * 2);
    const rsrc_t rVA = mk_rsrc(p.v[0] + ((size_t)g * SK + (SK - WIN)) * DH, (u32)WIN * DH * 2);
    const u32 id0 = (u32)(s * 8 + wave), id1 = id0 + 64;
    const u32 aoff = (u32)(sA * 512 + wave * 64) * (DH * 2);
    u32x4 ring[16];
    if (isA) {
#pragma unroll
      for (int m2 = 0; m2 < 2; ++m2) {
#pragma unroll
        for (int i = 0; i < 4; ++i) ring[m2 * 8 + i] = ld_a(rKA, voff + i * 1024, aoff + m2 * 4096);
#pragma unroll
        for (int i = 0; i < 4; ++i) ring[m2 * 8 + 4 + i] = ld_a(rVA, voff + i * 1024, aoff + m2 * 4096);
      }
    } else {
#pragma unroll
      for (int i = 0; i < 8; ++i) ring[i] = ld_x<AK>(rKB, voff + i * 1024, id0 * UNIT_BYTES);
#pragma unroll
      for (int i = 0; i < 8; ++i) ring[8 + i] = ld_x<AV>(rVB, voff + i * 1024, id0 * UNIT_BYTES);
    }
    FENCE();
    if (tid < 8) lds[L_FLAG + tid] = 0u;
    asm volatile("s_waitcnt lgkmcnt(0)" ::: "memory");
    __builtin_amdgcn_s_barrier();
    if (wave == 0) STAMP(0);
    {
      float a[8];
      const float x0 = bf_lo(xv[0]), x1 = bf_hi(xv[0]), x2 = bf_lo(xv[1]), x3 = bf_hi(xv[1]);
#pragma unroll
      for (int k = 0; k < 4; ++k) {
        a[2 * k] = x0 * bf_lo(wv[0][k]) + x1 * bf_lo(wv[1][k]) + x2 * bf_lo(wv[2][k]) + x3 * bf_lo(wv[3][k]);
        a[2 * k + 1] = x0 * bf_hi(wv[0][k]) + x1 * bf_hi(wv[1][k]) + x2 * bf_hi(wv[2][k]) + x3 * bf_hi(wv[3][k]);
      }
#pragma unroll
      for (int k = 0; k < 8; ++k) { float t = a[k]; t += dpp<ROR4>(t); t += dpp<XOR8>(t); a[k] = xrow_sum(t); }
      if (lane < 4) {
        *(LAS f32x4*)(ldsf + L_QRED + wave * 32 + lane * 8) = f32x4{a[0], a[1], a[2], a[3]};
        *(LAS f32x4*)(ldsf + L_QRED + wave * 32 + lane * 8 + 4) = f32x4{a[4], a[5], a[6], a[7]};
      }
      lds_signal_add(lds + L_FLAG + F_CNTQ, lane);
    }
    if (wave == 0) STAMP(1);
    if (isA) {
      lds_wait(lds + L_FLAG + F_QA, 1u);
      u32x4 qa[4];
#pragma unroll
      for (int j = 0; j < 4; ++j) qa[j] = *(LAS u32x4*)(lds + L_QA + j * 64 + 4 * c);
      float mA = -INFINITY, lA = 0.f, acc[4][8];
#pragma unroll
      for (int j = 0; j < 4; ++j)
#pragma unroll
        for (int k = 0; k < 8; ++k) acc[j][k] = 0.f;
#pragma unroll
      for (int mt = 0; mt < 4; ++mt) {
        const int sl = (mt & 1) * 8;
        float x[16];
#pragma unroll
        for (int t = 0; t < 4; ++t) {
          const u32x4 kk = ring[sl + t];
#pragma unroll
          for (int j = 0; j < 4; ++j) {
            float d = dot2(kk[0], qa[j][0], 0.f); d = dot2(kk[1], qa[j][1], d); d = dot2(kk[2], qa[j][2], d); d = dot2(kk[3], qa[j][3], d);
            x[4 * j + t] = d;
          }
          PIN4(x[t], x[4 + t], x[8 + t], x[12 + t]);
          if (mt < 2) ring[sl + t] = ld_a(rKA, voff + t * 1024, aoff + (mt + 2) * 4096);
          else if (mt == 2) ring[sl + t] = ld_x<AK>(rKB, voff + t * 1024, id0 * UNIT_BYTES);
          else ring[sl + t] = ld_x<AV>(rVB, voff + t * 1024, id0 * UNIT_BYTES);
          FENCE();
        }
        TR_STEP(8, XOR8, 8) TR_STEP(4, XOR7, 4) TR_STEP(2, XOR2, 2) TR_STEP(1, XOR1, 1)
        const float sc = x[0];
        float pm = fmaxf(sc, dpp<XOR1>(sc)); pm = fmaxf(pm, dpp<XOR2>(pm));
        const float mn = fmaxf(mA, pm);
        const float al = ex2((mA - mn) * C2), pp = ex2((sc - mn) * C2);
        lA = lA * al + pp; mA = mn;
        if (mt > 0) {
#pragma unroll
          for (int j = 0; j < 4; ++j) {
            const float aj = j == 0 ? dpp<BCAST + 0>(al) : j == 1 ? dpp<BCAST + 4>(al) : j == 2 ? dpp<BCAST + 8>(al) : dpp<BCAST + 12>(al);
#pragma unroll
            for (int k = 0; k < 8; ++k) acc[j][k] *= aj;
          }
        }
#define A_PV(T) { const u32x4 vv = ring[sl + 4 + (T)]; float vf[8]; \
          _Pragma("unroll") for (int k = 0; k < 4; ++k) { vf[2 * k] = bf_lo(vv[k]); vf[2 * k + 1] = bf_hi(vv[k]); } \
          const float p0 = dpp<BCAST + 0 + (T)>(pp), p1 = dpp<BCAST + 4 + (T)>(pp), p2 = dpp<BCAST + 8 + (T)>(pp), p3 = dpp<BCAST + 12 + (T)>(pp); \
          _Pragma("unroll") for (int k = 0; k < 8; ++k) { acc[0][k] += p0 * vf[k]; acc[1][k] += p1 * vf[k]; acc[2][k] += p2 * vf[k]; acc[3][k] += p3 * vf[k]; } \
          PIN8(acc[0]); PIN8(acc[1]); PIN8(acc[2]); PIN8(acc[3]); \
          if (mt < 2) ring[sl + 4 + (T)] = ld_a(rVA, voff + (T) * 1024, aoff + (mt + 2) * 4096); \
          else if (mt == 2) ring[sl + 4 + (T)] = ld_x<AK>(rKB, voff + (4 + (T)) * 1024, id0 * UNIT_BYTES); \
          else ring[sl + 4 + (T)] = ld_x<AV>(rVB, voff + (4 + (T)) * 1024, id0 * UNIT_BYTES); \
          FENCE(); }
        A_PV(0) A_PV(1) A_PV(2) A_PV(3)
#undef A_PV
      }
      const float M = xrow_max(mA);
      const float f = ex2((mA - M) * C2);
      float L = lA * f; L += dpp<XOR1>(L); L += dpp<XOR2>(L); L = xrow_sum(L);
#pragma unroll
      for (int j = 0; j < 4; ++j) {
        const float fj = j == 0 ? dpp<BCAST + 0>(f) : j == 1 ? dpp<BCAST + 4>(f) : j == 2 ? dpp<BCAST + 8>(f) : dpp<BCAST + 12>(f);
#pragma unroll
        for (int k = 0; k < 8; ++k) acc[j][k] = xrow_sum(acc[j][k] * fj);
      }
      if (r == 0) {
#pragma unroll
        for (int j = 0; j < 4; ++j) {
          LAS float* rec = ldsf + L_PA + (wave * 4 + j) * PREC;
          *(LAS f32x4*)(rec + 8 * c) = f32x4{acc[j][0], acc[j][1], acc[j][2], acc[j][3]};
          *(LAS f32x4*)(rec + 8 * c + 4) = f32x4{acc[j][4], acc[j][5], acc[j][6], acc[j][7]};
        }
        if ((c & 3) == 0) { LAS float* rec = ldsf + L_PA + (wave * 4 + (c >> 2)) * PREC; rec[128] = M; rec[129] = L; }
      }
      lds_signal_add(lds + L_FLAG + F_CNTA, lane);
      if (wave == 0) STAMP(3);
    }
    lds_wait(lds + L_FLAG + F_QB, 1u);
    if (wave == 0) STAMP(2);
    const u32x4 q = *(LAS u32x4*)(lds + L_QB + 4 * c);
    GAS u32* cnt = (GAS u32*)(p.ws + WS_CNT + (size_t)h * 256);
    float m = -INFINITY, l = 0.f, acc[8];
#pragma unroll
    for (int k = 0; k < 8; ++k) acc[k] = 0.f;
    u32 unext = id1;
    u32 claimv = claim(cnt, lane);
    FENCE();
    for (;;) {
      const u32 soff = (unext < NUNIT ? unext : NUNIT) * UNIT_BYTES;
      float x[8];
#pragma unroll
      for (int i = 0; i < 8; ++i) {
        const u32x4 kk = ring[i];
        float d = dot2(kk[0], q[0], 0.f); d = dot2(kk[1], q[1], d); d = dot2(kk[2], q[2], d); d = dot2(kk[3], q[3], d);
        PIN1(d);
        x[i] = d;
        ring[i] = ld_x<AK>(rKB, voff + i * 1024, soff);
        FENCE();
      }
      TR_STEP(4, XOR8, 8) TR_STEP(2, XOR7, 4) TR_STEP(1, XOR2, 2)
      const float sc = x[0] + dpp<XOR1>(x[0]);
      float pm = fmaxf(sc, dpp<XOR2>(sc)); pm = fmaxf(pm, dpp<XOR7>(pm)); pm = fmaxf(pm, dpp<XOR8>(pm));
      const float mn = fmaxf(m, pm);
      const float al = ex2((m - mn) * C2), pp = ex2((sc - mn) * C2);
      l = l * al + pp; m = mn;
#pragma unroll
      for (int k = 0; k < 8; ++k) acc[k] *= al;
#define B_PV(I) { const u32x4 vv = ring[8 + (I)]; const float pb = dpp<BCAST + 2 * (I)>(pp); \
        _Pragma("unroll") for (int k = 0; k < 4; ++k) { acc[2 * k] += pb * bf_lo(vv[k]); acc[2 * k + 1] += pb * bf_hi(vv[k]); } \
        PIN8(acc); \
        ring[8 + (I)] = ld_x<AV>(rVB, voff + (I) * 1024, soff); FENCE(); }
      B_PV(0) B_PV(1) B_PV(2) B_PV(3) B_PV(4) B_PV(5) B_PV(6) B_PV(7)
#undef B_PV
      if (unext >= NUNIT) break;
      unext = (u32)__builtin_amdgcn_readfirstlane((int)claimv) + NSTATIC;
      claimv = claim(cnt, lane);
      FENCE();
    }
    if (wave == 0) STAMP(4);
    {
      const float M = xrow_max(m);
      const float f = ex2((m - M) * C2);
      float L = l * f; L += dpp<XOR1>(L); L += dpp<XOR2>(L); L += dpp<XOR7>(L); L += dpp<XOR8>(L); L = xrow_sum(L) * 0.5f;
#pragma unroll
      for (int k = 0; k < 8; ++k) acc[k] = xrow_sum(acc[k] * f);
      if (r == 0) {
        LAS float* rec = ldsf + L_PB + wave * PREC;
        *(LAS f32x4*)(rec + 8 * c) = f32x4{acc[0], acc[1], acc[2], acc[3]};
        *(LAS f32x4*)(rec + 8 * c + 4) = f32x4{acc[4], acc[5], acc[6], acc[7]};
        if (c == 0) { rec[128] = M; rec[129] = L; }
      }
      lds_signal_add(lds + L_FLAG + F_CNTB, lane);
    }
}

__global__ void __launch_bounds__(NTHR) fused_decode_kernel(Params p) {
  __shared__ __attribute__((aligned(16))) u32 lds_raw[L_WORDS];
  LAS u32* lds = (LAS u32*)lds_raw;
  LAS float* ldsf = (LAS float*)lds_raw;
  const int tid = threadIdx.x, lane = tid & 63;
  const int wave = __builtin_amdgcn_readfirstlane(tid >> 6);
  const int b = blockIdx.x;
  const int h = b >> 3, s = b & 7;
  const bool isA = ((s & 3) == (h & 3));
  const int g = h >> 2;
  const int sA = (h & 3) * 2 + (s >> 2);
  const int cb = (b & 7) * 32 + (b >> 3);
  const int brq = cb >> 7, col0 = (cb & 127) * 32;

  if (wave < NSW) {
    stream_waves<AUX_K, AUX_V>(p, lds, ldsf, tid, lane, wave, b, h, s, isA, g, sA, brq, col0);
    return;
  }

  const int br = wave - NSW;
  const rsrc_t rWS = mk_rsrc(p.ws, (u32)WS_END);
  u32 tag = 0, ep = 0;
  if (br == 1) ep = __hip_atomic_load((GAS u32*)(p.ws + WS_EPOCH), RLX_AGENT);
  __builtin_amdgcn_sched_barrier(0);
  __builtin_amdgcn_s_barrier();
  __builtin_amdgcn_sched_barrier(0);
  if (br == 1) {
    asm volatile("" : "+v"(ep));
    tag = ep * 1664525u + 1013904223u;
    if (lane == 0) { lds[L_FLAG + F_TAG] = tag; }
    lds_signal_add(lds + L_FLAG + F_TAGOK, lane);
    lds_wait(lds + L_FLAG + F_CNTQ, (u32)NSW);
    float qs = 0.f;
    if (lane < 32) {
#pragma unroll
      for (int w = 0; w < NSW; ++w) qs += ldsf[L_QRED + w * 32 + lane];
    }
    const float qn = dpp<XOR1>(qs);
    const u32 pair = pack_bf16(qs, qn);
    if (brq == 1 && (col0 & 127) == 0) {
      if (lane == 0) __hip_atomic_store((GAS u32*)(p.ws + WS_CNT + (size_t)(col0 >> 7) * 256), 0u, RLX_AGENT);
      asm volatile("s_waitcnt vmcnt(0)" ::: "memory");
    }
    if (lane < 32 && (lane & 1) == 0) {
      GAS u64* dst = (GAS u64*)(p.ws + WS_QG) + (size_t)(brq * 32 + (col0 >> 7)) * 64 + ((col0 & 127) >> 1) + (lane >> 1);
      __hip_atomic_store(dst, ((u64)tag << 32) | pair, RLX_AGENT);
    }
    STAMP(8);
    GAS u64* src = (GAS u64*)(p.ws + WS_QG) + (size_t)(32 + h) * 64 + lane;
    u64 gq;
    for (u32 spins = 0;; ++spins) {
      gq = __hip_atomic_load(src, RLX_AGENT);
      if (__all((u32)(gq >> 32) == tag) || spins > SPIN_CAP) break;
      __builtin_amdgcn_s_sleep(2);
    }
    lds[L_QB + lane] = (u32)gq;
    lds_signal_add(lds + L_FLAG + F_QB, lane);
    STAMP(9);
  } else {
    lds_wait(lds + L_FLAG + F_TAGOK, 1u);
    tag = lds[L_FLAG + F_TAG];
    if (isA) {
      GAS u64* src = (GAS u64*)(p.ws + WS_QG) + (size_t)(4 * g) * 64 + lane;
      u64 gq[4];
      for (u32 spins = 0;; ++spins) {
        bool ok = true;
#pragma unroll
        for (int j = 0; j < 4; ++j) { gq[j] = __hip_atomic_load(src + j * 64, RLX_AGENT); ok &= (u32)(gq[j] >> 32) == tag; }
        if (__all(ok) || spins > SPIN_CAP) break;
        __builtin_amdgcn_s_sleep(2);
      }
#pragma unroll
      for (int j = 0; j < 4; ++j) lds[L_QA + j * 64 + lane] = (u32)gq[j];
      lds_signal_add(lds + L_FLAG + F_QA, lane);
    }
  }
  const int rq = lane >> 3, cg = lane & 7;
  u32 wp[8][8];
  {
    const GAS u32x4* w = (const GAS u32x4*)(p.wo[br] + ((size_t)h * DH + 2 * rq) * DM + s * 64 + cg * 8);
    u32x4 wa[8], wb[8];
#pragma unroll
    for (int i = 0; i < 8; ++i) {
      wa[i] = __builtin_nontemporal_load(w + (size_t)(16 * i) * (DM / 8));
      wb[i] = __builtin_nontemporal_load(w + (size_t)(16 * i + 1) * (DM / 8));
    }
#pragma unroll
    for (int i = 0; i < 8; ++i)
#pragma unroll
      for (int k = 0; k < 4; ++k) {
        wp[i][2 * k] = __builtin_amdgcn_perm(wb[i][k], wa[i][k], 0x05040100u);
        wp[i][2 * k + 1] = __builtin_amdgcn_perm(wb[i][k], wa[i][k], 0x07060302u);
      }
  }
  if (br == 1 || isA) {
    lds_wait(lds + L_FLAG + (br ? F_CNTB : F_CNTA), (u32)NSW);
    if (br == 1) STAMP(10);
    const int nh = br ? 1 : 4;
    for (int j = 0; j < nh; ++j) {
      const LAS float* rec = ldsf + (br ? L_PB : L_PA + j * PREC);
      const int stride = br ? PREC : 4 * PREC;
      float mw[NSW], M = -INFINITY;
#pragma unroll
      for (int w = 0; w < NSW; ++w) { mw[w] = rec[w * stride + 128]; M = fmaxf(M, mw[w]); }
      float L = 0.f, o0 = 0.f, o1 = 0.f;
#pragma unroll
      for (int w = 0; w < NSW; ++w) {
        const float f = ex2((mw[w] - M) * C2);
        const f32x2 o = *(const LAS f32x2*)(rec + w * stride + 2 * lane);
        L += f * rec[w * stride + 129]; o0 += f * o[0]; o1 += f * o[1];
      }
      const u32 slot = br ? (u32)((32 + h) * 8 + s) : (u32)((4 * g + j) * 8 + sA);
      st_sc1(u32x4{__float_as_uint(o0), tag, __float_as_uint(o1), tag}, rWS, (u32)WS_PO + slot * 1024u + (u32)lane * 16u, 0);
      if (lane == 0) st_sc1(u32x4{__float_as_uint(M * C2), tag, __float_as_uint(L), tag}, rWS, (u32)WS_PML + slot * 16u, 0);
    }
  }
  if (br == 1) STAMP(11);
  {
    const u32 slot0 = (u32)((br * 32 + h) * 8);
    u32x4 po[8], ml;
    for (u32 spins = 0;; ++spins) {
      asm volatile("" ::: "memory");
      ml = ld_sc1(rWS, (u32)WS_PML + (slot0 + (u32)(lane & 7)) * 16u, 0);
#pragma unroll
      for (int sp = 0; sp < 8; ++sp) po[sp] = ld_sc1(rWS, (u32)WS_PO + (slot0 + sp) * 1024u + (u32)lane * 16u, 0);
      bool ok = (ml[1] == tag) & (ml[3] == tag);
#pragma unroll
      for (int sp = 0; sp < 8; ++sp) ok &= (po[sp][1] == tag) & (po[sp][3] == tag);
      if (__all(ok) || spins > SPIN_CAP) break;
      if (br) __builtin_amdgcn_s_sleep(1); else __builtin_amdgcn_s_sleep(8);
    }
    if (br == 1) STAMP(12);
    const float ms = __uint_as_float(ml[0]);
    float M = fmaxf(ms, dpp<XOR1>(ms)); M = fmaxf(M, dpp<XOR2>(M)); M = fmaxf(M, dpp<XOR7>(M));
    const float f = ex2(ms - M);
    float L = f * __uint_as_float(ml[2]); L += dpp<XOR1>(L); L += dpp<XOR2>(L); L += dpp<XOR7>(L);
    float o0 = 0.f, o1 = 0.f;
#pragma unroll
    for (int sp = 0; sp < 8; ++sp) {
      const float fs = __builtin_bit_cast(float, __builtin_amdgcn_readlane(__builtin_bit_cast(int, f), sp));
      o0 += fs * __uint_as_float(po[sp][0]); o1 += fs * __uint_as_float(po[sp][2]);
    }
    const float inv = 1.0f / L;
    lds[L_OB + br * 64 + lane] = pack_bf16(o0 * inv, o1 * inv);
    asm volatile("s_waitcnt lgkmcnt(0)" ::: "memory");
    float y[8];
#pragma unroll
    for (int j = 0; j < 8; ++j) y[j] = 0.f;
#pragma unroll
    for (int i = 0; i < 8; ++i) {
      const u32 ob = lds[L_OB + br * 64 + rq + 8 * i];
#pragma unroll
      for (int j = 0; j < 8; ++j) y[j] = dot2(wp[i][j], ob, y[j]);
    }
#pragma unroll
    for (int j = 0; j < 4; ++j) {
      const float t1 = y[j] + dpp<XOR8>(y[j]), t2 = y[j + 4] + dpp<XOR8>(y[j + 4]);
      y[j] = (lane & 8) ? t2 : t1;
    }
#pragma unroll
    for (int j = 0; j < 2; ++j) {
      auto sw = __builtin_amdgcn_permlane16_swap(__float_as_uint(y[j]), __float_as_uint(y[j + 2]), false, false);
      y[j] = __uint_as_float(sw[0]) + __uint_as_float(sw[1]);
    }
    auto sw = __builtin_amdgcn_permlane32_swap(__float_as_uint(y[0]), __float_as_uint(y[1]), false, false);
    const float yy = __uint_as_float(sw[0]) + __uint_as_float(sw[1]);
    const int col = 8 * cg + ((lane >> 3) & 1) * 4 + ((lane >> 4) & 1) * 2 + (lane >> 5);
    GAS u64* dst = (GAS u64*)(p.ws + WS_OP) + (size_t)(br * 32 + h) * 512 + s * 64 + col;
    __hip_atomic_store(dst, ((u64)tag << 32) | __float_as_uint(yy), RLX_AGENT);
  }
  if (br == 1) STAMP(13);
  {
    GAS u64* src = (GAS u64*)(p.ws + WS_OP) + (size_t)(br * 32 + (lane >> 1)) * 512 + 2 * b + (lane & 1);
    u64 gy;
    for (u32 spins = 0;; ++spins) {
      gy = __hip_atomic_load(src, RLX_AGENT);
      if (__all((u32)(gy >> 32) == tag) || spins > SPIN_CAP) break;
      if (br) __builtin_amdgcn_s_sleep(1); else __builtin_amdgcn_s_sleep(8);
    }
    float y = __uint_as_float((u32)gy);
    y += dpp<XOR2>(y); y += dpp<ROR4>(y); y += dpp<XOR8>(y);
    y = xrow_sum(y);
    const float yn = dpp<XOR1>(y);
    if (lane == 0) *(GAS u32*)(p.out + br * 512 + 2 * b) = pack_bf16(y, yn);
    if (br == 1 && b == 0 && lane == 0) __hip_atomic_store((GAS u32*)(p.ws + WS_EPOCH), tag, RLX_AGENT);
  }
  if (br == 1) STAMP(14);
}

}

extern "C" void kernel_launch(void* const* d_in, const int* in_sizes, int n_in, void* d_out, int out_size, void* d_ws,
                              size_t ws_size, hipStream_t stream) {
  Params p;
  p.x[0] = (const uint16_t*)d_in[0]; p.k[0] = (const uint16_t*)d_in[1]; p.v[0] = (const uint16_t*)d_in[2];
  p.wq[0] = (const uint16_t*)d_in[3]; p.wo[0] = (const uint16_t*)d_in[4];
  p.x[1] = (const uint16_t*)d_in[5]; p.k[1] = (const uint16_t*)d_in[6]; p.v[1] = (const uint16_t*)d_in[7];
  p.wq[1] = (const uint16_t*)d_in[8]; p.wo[1] = (const uint16_t*)d_in[9];
  p.out = (uint16_t*)d_out; p.ws = (unsigned char*)d_ws;
  hipLaunchKernelGGL(fused_decode_kernel, dim3(NWG), dim3(NTHR), 0, stream, p);
}
```

```cpp
#include <hip/hip_runtime.h>
#include <hip/hip_bf16.h>
#include <stdint.h>

typedef unsigned u32;
typedef unsigned long long u64;
typedef u32 u32x4 __attribute__((ext_vector_type(4)));
typedef u32 u32x2 __attribute__((ext_vector_type(2)));
typedef float f32x4 __attribute__((ext_vector_type(4)));
typedef float f32x2 __attribute__((ext_vector_type(2)));
typedef __bf16 bf16x2 __attribute__((ext_vector_type(2)));
#define GAS __attribute__((address_space(1)))
#define LAS __attribute__((address_space(3)))
#define RLX_AGENT __ATOMIC_RELAXED, __HIP_MEMORY_SCOPE_AGENT
#define RLX_WG __ATOMIC_RELAXED, __HIP_MEMORY_SCOPE_WORKGROUP

namespace {

constexpr int DM = 512, NH = 32, DH = 128, SK = 16384, WIN = 4096, HKV = 8;
constexpr int NWG = 256, NSW = 8, NTHR = 640;
constexpr int QCOLS = NH * DH;
constexpr int NV = 4;
constexpr int HS = 2 * NV;
constexpr u32 UNIT_BYTES = 16 * DH * 2;
constexpr int WIN_UNITS = 3;
constexpr int AW = 2;
constexpr int GBLK = 4;
constexpr int RS = WIN_UNITS * HS;
constexpr int PRE = 4;
constexpr u32 NUNIT = SK / 16;
constexpr u32 EXTRA1 = 40;
constexpr u32 R2 = 8 * 64 + 6 * EXTRA1;
constexpr u32 Q1A = 64, Q1N = 64 + EXTRA1;
constexpr int CLAIM_LEAD = 1;
constexpr u32 NBLK2 = (NUNIT - R2) / GBLK;
constexpr u32 UQ_EMPTY = 0xffffffffu;
constexpr int UQ_N = Q1N + (NUNIT - R2) + 64 + 8 * GBLK;
constexpr float SCALE = 0.088388347648318440f;
constexpr float C2 = 1.4426950408889634f;
constexpr u32 SPIN_CAP = 1u << 18;

constexpr size_t WS_EPOCH = 0;
constexpr size_t WS_CNT = 4096;
constexpr size_t WS_QG = 16384;
constexpr size_t WS_PO = 65536;
constexpr size_t WS_PML = WS_PO + (size_t)2 * 32 * 8 * 128 * 8;
constexpr size_t WS_OP = WS_PML + (size_t)2 * 32 * 8 * 16;
constexpr size_t WS_END = WS_OP + (size_t)2 * 32 * 512 * 8;

struct Params {
  const uint16_t* x[2];
  const uint16_t* k[2];
  const uint16_t* v[2];
  const uint16_t* wq[2];
  const uint16_t* wo[2];
  uint16_t* out;
  unsigned char* ws;
};

constexpr int PREC = 132;
constexpr int L_QRED = 0;
constexpr int L_QB = L_QRED + 8 * 32;
constexpr int L_QA = L_QB + 64;
constexpr int L_OB = L_QA + 256;
constexpr int L_PB = L_OB + 128;
constexpr int L_PA = L_PB + 8 * PREC;
constexpr int L_FLAG = L_PA + 32 * PREC;
constexpr int L_BQ = L_FLAG + 8;
constexpr int L_WORDS = L_BQ + UQ_N;
enum { F_QB = 1, F_QA = 2, F_CNTA = 3, F_CNTB = 4, F_TAGOK = 5, F_PROG = 7 };

__device__ __forceinline__ float dot2(u32 a, u32 b, float acc) {
  return __builtin_amdgcn_fdot2_f32_bf16(__builtin_bit_cast(bf16x2, a), __builtin_bit_cast(bf16x2, b), acc, false);
}
template <int CTRL> __device__ __forceinline__ float dpp(float x) {
  return __builtin_bit_cast(float, __builtin_amdgcn_mov_dpp(__builtin_bit_cast(int, x), CTRL, 0xf, 0xf, true));
}
constexpr int XOR3 = 0x1B, XOR1 = 0xB1, XOR2 = 0x4E, XOR7 = 0x141, XOR8 = 0x128, ROR4 = 0x124, BCAST = 0x150;
__device__ __forceinline__ float xrow_max(float x) {
  auto s = __builtin_amdgcn_permlane16_swap(__float_as_uint(x), __float_as_uint(x), false, false);
  x = fmaxf(__uint_as_float(s[0]), __uint_as_float(s[1]));
  auto t = __builtin_amdgcn_permlane32_swap(__float_as_uint(x), __float_as_uint(x), false, false);
  return fmaxf(__uint_as_float(t[0]), __uint_as_float(t[1]));
}
__device__ __forceinline__ float xrow_sum(float x) {
  auto s = __builtin_amdgcn_permlane16_swap(__float_as_uint(x), __float_as_uint(x), false, false);
  x = __uint_as_float(s[0]) + __uint_as_float(s[1]);
  auto t = __builtin_amdgcn_permlane32_swap(__float_as_uint(x), __float_as_uint(x), false, false);
  return __uint_as_float(t[0]) + __uint_as_float(t[1]);
}
__device__ __forceinline__ float bf_lo(u32 w) { return __uint_as_float(w << 16); }
__device__ __forceinline__ float bf_hi(u32 w) { return __uint_as_float(w & 0xffff0000u); }
__device__ __forceinline__ u32 pack_bf16(float lo, float hi) {
  const __hip_bfloat16 a = __float2bfloat16(lo), b = __float2bfloat16(hi);
  return (u32)__builtin_bit_cast(uint16_t, a) | ((u32)__builtin_bit_cast(uint16_t, b) << 16);
}
__device__ __forceinline__ float score_of(float d) { return bf_lo(pack_bf16(bf_lo(pack_bf16(d, d)) * SCALE, 0.f)); }
__device__ __forceinline__ float ex2(float x) { return __builtin_amdgcn_exp2f(x); }

__device__ __forceinline__ u32 ep_request(const unsigned char* ws) { u32 e; asm volatile("s_load_dword %0, %1, 0x0" : "=s"(e) : "s"(ws)); return e; }
__device__ __forceinline__ u32 ep_tag(u32 e) {
  asm volatile("s_waitcnt lgkmcnt(0)" : "+s"(e));
  return e * 1664525u + 1013904223u;
}
__device__ __forceinline__ void lds_wait(LAS u32* w, u32 want) {
  while (__hip_atomic_load(w, RLX_WG) < want) __builtin_amdgcn_s_sleep(1);
  asm volatile("" ::: "memory");
}
__device__ __forceinline__ void lds_signal_add(LAS u32* w, int lane) {
  asm volatile("s_waitcnt lgkmcnt(0)" ::: "memory");
  if (lane == 0) __hip_atomic_fetch_add(w, 1u, RLX_WG);
}

#define FENCE() do { asm volatile("" ::: "memory"); __builtin_amdgcn_sched_barrier(0); } while (0)
#define PIN1(a) asm volatile("" : "+v"(a) :: "memory")
#define PIN4(a, b, c, d) asm volatile("" : "+v"(a), "+v"(b), "+v"(c), "+v"(d) :: "memory")
#define PIN8(a) asm volatile("" : "+v"((a)[0]), "+v"((a)[1]), "+v"((a)[2]), "+v"((a)[3]), "+v"((a)[4]), "+v"((a)[5]), "+v"((a)[6]), "+v"((a)[7]) :: "memory")
typedef __amdgpu_buffer_rsrc_t rsrc_t;
__device__ __forceinline__ rsrc_t mk_rsrc(const void* base, u32 bytes) {
  return __builtin_amdgcn_make_buffer_rsrc(const_cast<void*>(base), 0, bytes, 0x00020000);
}
__device__ __forceinline__ u32x4 ld_nt(rsrc_t r, u32 voff, u32 soff) { return __builtin_amdgcn_raw_buffer_load_b128(r, voff, soff, 2); }
__device__ __forceinline__ u32x4 ld_sc1(rsrc_t r, u32 voff, u32 soff) { return __builtin_amdgcn_raw_buffer_load_b128(r, voff, soff, 16); }
__device__ __forceinline__ void st_sc1(u32x4 v, rsrc_t r, u32 voff, u32 soff) { __builtin_amdgcn_raw_buffer_store_b128(v, r, voff, soff, 16); }

template <int SLEEP> __device__ __forceinline__ u64 poll_granule(GAS u64* src, const u32 tag) {
  u64 g0 = __hip_atomic_load(src, RLX_AGENT); __builtin_amdgcn_s_sleep(SLEEP); FENCE();
  u64 g1 = __hip_atomic_load(src, RLX_AGENT); __builtin_amdgcn_s_sleep(SLEEP); FENCE();
  u64 g2 = __hip_atomic_load(src, RLX_AGENT); __builtin_amdgcn_s_sleep(SLEEP); FENCE();
  u64 r;
  for (u32 spins = 0;; ++spins) {
    r = g0; if (__all((u32)(r >> 32) == tag)) break;
    g0 = __hip_atomic_load(src, RLX_AGENT); FENCE();
    r = g1; if (__all((u32)(r >> 32) == tag)) break;
    g1 = __hip_atomic_load(src, RLX_AGENT); FENCE();
    r = g2; if (__all((u32)(r >> 32) == tag) || spins > SPIN_CAP) break;
    g2 = __hip_atomic_load(src, RLX_AGENT); FENCE();
  }
  return r;
}

template <int N> __device__ __forceinline__ float tr_reduce(float (&x)[N], const int c) {
  if constexpr (N >= 2) {
    constexpr int H = N / 2;
#pragma unroll
    for (int i = 0; i < H; ++i) { const float t1 = x[i] + dpp<XOR8>(x[i]), t2 = x[i + H] + dpp<XOR8>(x[i + H]); x[i] = (c & 8) ? t2 : t1; }
  }
  if constexpr (N >= 4) {
    constexpr int H = N / 4;
#pragma unroll
    for (int i = 0; i < H; ++i) { const float t1 = x[i] + dpp<XOR7>(x[i]), t2 = x[i + H] + dpp<XOR7>(x[i + H]); x[i] = (c & 4) ? t2 : t1; }
  } else x[0] += dpp<XOR7>(x[0]);
  if constexpr (N >= 8) {
    constexpr int H = N / 8;
#pragma unroll
    for (int i = 0; i < H; ++i) { const float t1 = x[i] + dpp<XOR2>(x[i]), t2 = x[i + H] + dpp<XOR2>(x[i + H]); x[i] = (c & 2) ? t2 : t1; }
  } else x[0] += dpp<XOR2>(x[0]);
  if constexpr (N >= 16) { const float t1 = x[0] + dpp<XOR1>(x[0]), t2 = x[1] + dpp<XOR1>(x[1]); x[0] = (c & 1) ? t2 : t1; }
  else x[0] += dpp<XOR1>(x[0]);
  return x[0];
}

template <int I, bool REFILL> struct PV {
  static __device__ __forceinline__ void go(u32x4* half, const float pp, float (&acc)[8], const rsrc_t rV, const u32 voff, const u32 soff) {
    const u32x4 vv = half[NV + I];
    const float pb = dpp<BCAST + I * (16 / NV)>(pp);
#pragma unroll
    for (int k = 0; k < 4; ++k) { acc[2 * k] += pb * bf_lo(vv[k]); acc[2 * k + 1] += pb * bf_hi(vv[k]); }
    PIN8(acc);
    if constexpr (REFILL) half[NV + I] = ld_nt(rV, voff + I * 1024, soff);
    FENCE();
    if constexpr (I + 1 < NV) PV<I + 1, REFILL>::go(half, pp, acc, rV, voff, soff);
  }
};
template <bool REFILL>
__device__ __forceinline__ void b_step(u32x4* half, const u32x4 q, float& m, float& l, float (&acc)[8], const rsrc_t rK, const rsrc_t rV, const u32 voff, const u32 soff, const int c, const bool valid) {
  float x[NV];
#pragma unroll
  for (int i = 0; i < NV; ++i) {
    const u32x4 kk = half[i];
    float d = dot2(kk[0], q[0], 0.f); d = dot2(kk[1], q[1], d); d = dot2(kk[2], q[2], d); d = dot2(kk[3], q[3], d);
    PIN1(d);
    x[i] = d;
    if constexpr (REFILL) half[i] = ld_nt(rK, voff + i * 1024, soff);
    FENCE();
  }
  const float sc0 = tr_reduce<NV>(x, c);
  const float sc = valid ? score_of(sc0) : -INFINITY;
  float pm = fmaxf(sc, dpp<XOR8>(sc));
  pm = fmaxf(pm, dpp<XOR7>(pm));
  const float mn = fmaxf(m, pm);
  const float al = ex2((m - mn) * C2), pp = ex2((sc - mn) * C2);
  l = l * al + pp; m = mn;
#pragma unroll
  for (int k = 0; k < 8; ++k) acc[k] *= al;
  PV<0, REFILL>::go(half, pp, acc, rV, voff, soff);
}
__device__ __forceinline__ u32 p1_unit(const u32 t, const int s, const int h) {
  const int na = (s > (h & 3) ? 1 : 0) + (s > (h & 3) + 4 ? 1 : 0);
  return (u32)s * Q1N - (u32)na * (Q1N - Q1A) + t;
}
template <int X, int XEND> struct FirstLoads {
  static __device__ __forceinline__ void go(u32x4* ring, const rsrc_t rK, const rsrc_t rV, const u32 voff, const u32 (&so)[WIN_UNITS]) {
    if constexpr (X < XEND) {
      constexpr int u = X / 8, k = X % 8;
      ring[X] = ld_nt(k < 4 ? rK : rV, voff + (k % 4) * 1024, so[u]);
      FENCE();
      FirstLoads<X + 1, XEND>::go(ring, rK, rV, voff, so);
    }
  }
};
template <int X> struct ExtLoads {
  static __device__ __forceinline__ void go(u32x4* ring, const rsrc_t rK, const rsrc_t rV, const u32 voff, const u32 (&so)[WIN_UNITS]) {
    if constexpr (X < RS) {
      asm volatile("s_waitcnt vmcnt(1)" ::: "memory");
      FirstLoads<X, X + 4>::go(ring, rK, rV, voff, so);
      ExtLoads<X + 4>::go(ring, rK, rV, voff, so);
    }
  }
};
template <int MT> __device__ __forceinline__ void a_step(u32x4* ring, const u32x4 (&qa)[4], float& mA, float& lA, float (&acc)[4][8], const rsrc_t rKA, const rsrc_t rVA,
                                                         const rsrc_t rKB, const rsrc_t rVB, const u32 voff, const u32 aoff, const u32 (&sob)[WIN_UNITS], const int c) {
  constexpr int sl = (MT % AW) * 8;
  constexpr bool more = MT + AW < 4;
  const u32 snext = more ? aoff + (MT + AW) * 8 * 4096 : sob[MT % AW];
  const rsrc_t nK = more ? rKA : rKB, nV = more ? rVA : rVB;
  float x[16];
#define A_QK(T) { const u32x4 kk = ring[sl + (T)]; \
    _Pragma("unroll") for (int j = 0; j < 4; ++j) { \
      float d = dot2(kk[0], qa[j][0], 0.f); d = dot2(kk[1], qa[j][1], d); d = dot2(kk[2], qa[j][2], d); d = dot2(kk[3], qa[j][3], d); x[4 * j + (T)] = d; } \
    PIN4(x[(T)], x[4 + (T)], x[8 + (T)], x[12 + (T)]); \
    ring[sl + (T)] = ld_nt(nK, voff + (T) * 1024, snext); \
    FENCE(); }
  A_QK(0) A_QK(1) A_QK(2) A_QK(3)
#undef A_QK
  const float sc = score_of(tr_reduce<16>(x, c));
  float pm = fmaxf(sc, dpp<XOR1>(sc)); pm = fmaxf(pm, dpp<XOR2>(pm));
  const float mn = fmaxf(mA, pm);
  const float al = ex2((mA - mn) * C2), pp = ex2((sc - mn) * C2);
  lA = lA * al + pp; mA = mn;
  if constexpr (MT > 0) {
    const float a0 = dpp<BCAST + 0>(al), a1 = dpp<BCAST + 4>(al), a2 = dpp<BCAST + 8>(al), a3 = dpp<BCAST + 12>(al);
#pragma unroll
    for (int k = 0; k < 8; ++k) { acc[0][k] *= a0; acc[1][k] *= a1; acc[2][k] *= a2; acc[3][k] *= a3; }
  }
#define A_PV(T) { const u32x4 vv = ring[sl + 4 + (T)]; float vf[8]; \
    _Pragma("unroll") for (int k = 0; k < 4; ++k) { vf[2 * k] = bf_lo(vv[k]); vf[2 * k + 1] = bf_hi(vv[k]); } \
    const float p0 = dpp<BCAST + 0 + (T)>(pp), p1 = dpp<BCAST + 4 + (T)>(pp), p2 = dpp<BCAST + 8 + (T)>(pp), p3 = dpp<BCAST + 12 + (T)>(pp); \
    _Pragma("unroll") for (int k = 0; k < 8; ++k) { acc[0][k] += p0 * vf[k]; acc[1][k] += p1 * vf[k]; acc[2][k] += p2 * vf[k]; acc[3][k] += p3 * vf[k]; } \
    PIN8(acc[0]); PIN8(acc[1]); PIN8(acc[2]); PIN8(acc[3]); \
    ring[sl + 4 + (T)] = ld_nt(nV, voff + (T) * 1024, snext); \
    FENCE(); }
  A_PV(0) A_PV(1) A_PV(2) A_PV(3)
#undef A_PV
}
__device__ __forceinline__ u32 next_unit(LAS u32* lds, const int lane, bool& valid) {
  u32 t = 0;
  if (lane == 0) t = __hip_atomic_fetch_add(lds + L_FLAG + F_PROG, 1u, RLX_WG);
  t = (u32)__builtin_amdgcn_readfirstlane((int)t);
  u32 id = (u32)__builtin_amdgcn_readfirstlane((int)__hip_atomic_load(lds + L_BQ + t, RLX_WG));
  while (id == UQ_EMPTY) { __builtin_amdgcn_s_sleep(1); id = (u32)__builtin_amdgcn_readfirstlane((int)__hip_atomic_load(lds + L_BQ + t, RLX_WG)); }
  valid = id < NUNIT;
  return id * UNIT_BYTES;
}

__global__ void __launch_bounds__(NTHR) fused_decode_kernel(Params p) {
  __shared__ __attribute__((aligned(16))) u32 lds_raw[L_WORDS];
  LAS u32* lds = (LAS u32*)lds_raw;
  LAS float* ldsf = (LAS float*)lds_raw;
  const int tid = threadIdx.x, lane = tid & 63;
  const int wave = __builtin_amdgcn_readfirstlane(tid >> 6);
  const int b = blockIdx.x;
  const int h = b >> 3, s = b & 7;
  const bool isA = ((s & 3) == (h & 3));
  const int g = h >> 2;
  const int sA = (h & 3) * 2 + (s >> 2);
  const int cb = (b & 7) * 32 + (b >> 3);
  const int brq = cb >> 7, col0 = (cb & 127) * 32;
  u64 pc0;
  asm volatile("s_getpc_b64 %0\nLK_START:" : "=s"(pc0));
  u32x4 cpf0, cpf1;
  {
    u32 len, l2;
    asm volatile("s_mov_b32 %0, LK_END1-LK_START\n\ts_mov_b32 %1, LK_END2-LK_START\n\ts_max_u32 %0, %0, %1\n\ts_mov_b32 %1, LK_END3-LK_START\n\ts_max_u32 %0, %0, %1"
                 : "=s"(len), "=s"(l2) :: "scc");
    const rsrc_t rc = mk_rsrc((const void*)pc0, len & ~15u);
    const u32 piece = h < 2 ? (u32)((h & 1) * 20 + wave * 2) : 0x100000u;
    cpf0 = __builtin_amdgcn_raw_buffer_load_b128(rc, (u32)lane * 16u, piece * 1024u, 0);
    cpf1 = __builtin_amdgcn_raw_buffer_load_b128(rc, (u32)lane * 16u + 1024u, piece * 1024u, 0);
  }
  asm volatile("" :: "s"(p.x[0]), "s"(p.x[1]), "s"(p.k[0]), "s"(p.k[1]), "s"(p.v[0]), "s"(p.v[1]), "s"(p.wq[0]), "s"(p.wq[1]), "s"(p.wo[0]), "s"(p.wo[1]), "s"(p.out), "s"(p.ws));
  const int r = lane >> 4, c = lane & 15;
  const u32 voff = (u32)lane * 16u;
  const rsrc_t rKB = mk_rsrc(p.k[1] + (size_t)h * SK * DH, (u32)SK * DH * 2);
  const rsrc_t rVB = mk_rsrc(p.v[1] + (size_t)h * SK * DH, (u32)SK * DH * 2);
  const rsrc_t rKA = mk_rsrc(p.k[0] + ((size_t)g * SK + (SK - WIN)) * DH, (u32)WIN * DH * 2);
  const rsrc_t rVA = mk_rsrc(p.v[0] + ((size_t)g * SK + (SK - WIN)) * DH, (u32)WIN * DH * 2);
  const u32 aoff = (u32)(sA * 512) * (DH * 2) + (u32)wave * 4096u;
  u32 sob[WIN_UNITS], sof[WIN_UNITS];
#pragma unroll
  for (int u = 0; u < WIN_UNITS; ++u) { sob[u] = p1_unit((u32)(u * 8 + wave), s, h) * UNIT_BYTES; sof[u] = isA ? aoff + u * 8 * 4096 : sob[u]; }
  const rsrc_t rWS = mk_rsrc(p.ws, (u32)WS_END);
  u32x4 ring[RS];
  u32 tag = 0;
  rsrc_t fK, fV;
  if (__builtin_expect(wave < NSW, 1)) {
    const int rg = tid >> 2, cq = tid & 3;
    const uint16_t* xq = brq ? p.x[1] : p.x[0];
    const uint16_t* wqq = brq ? p.wq[1] : p.wq[0];
    const u32x2 xv = *(const GAS u32x2*)(xq + 4 * rg);
    u32x4 wv[4];
    {
      const GAS u32x4* wp = (const GAS u32x4*)(wqq + (size_t)(4 * rg) * QCOLS + col0 + 8 * cq);
#pragma unroll
      for (int i = 0; i < 4; ++i) wv[i] = __builtin_nontemporal_load(wp + (size_t)i * (QCOLS / 8));
    }
    FENCE();
    const uint16_t* pKA = p.k[0] + ((size_t)g * SK + (SK - WIN)) * DH; const uint16_t* pVA = p.v[0] + ((size_t)g * SK + (SK - WIN)) * DH;
    const uint16_t* pKB = p.k[1] + (size_t)h * SK * DH; const uint16_t* pVB = p.v[1] + (size_t)h * SK * DH;
    const u32 fbytes = isA ? (u32)WIN * DH * 2 : (u32)SK * DH * 2;
    fK = mk_rsrc(isA ? pKA : pKB, fbytes); fV = mk_rsrc(isA ? pVA : pVB, fbytes);
    FirstLoads<0, PRE>::go(ring, fK, fV, voff, sof);
    float a[8];
#pragma unroll
    for (int k = 0; k < 4; ++k) {
      a[2 * k] = dot2(__builtin_amdgcn_perm(wv[3][k], wv[2][k], 0x05040100u), xv[1], dot2(__builtin_amdgcn_perm(wv[1][k], wv[0][k], 0x05040100u), xv[0], 0.f));
      a[2 * k + 1] = dot2(__builtin_amdgcn_perm(wv[3][k], wv[2][k], 0x07060302u), xv[1], dot2(__builtin_amdgcn_perm(wv[1][k], wv[0][k], 0x07060302u), xv[0], 0.f));
    }
#pragma unroll
    for (int j = 0; j < 4; ++j) { const float t1 = a[j] + dpp<XOR8>(a[j]), t2 = a[j + 4] + dpp<XOR8>(a[j + 4]); a[j] = (lane & 8) ? t2 : t1; }
#pragma unroll
    for (int j = 0; j < 2; ++j) {
      auto sw = __builtin_amdgcn_permlane16_swap(__float_as_uint(a[j]), __float_as_uint(a[j + 2]), false, false);
      a[j] = __uint_as_float(sw[0]) + __uint_as_float(sw[1]);
    }
    auto sw = __builtin_amdgcn_permlane32_swap(__float_as_uint(a[0]), __float_as_uint(a[1]), false, false);
    float aa = __uint_as_float(sw[0]) + __uint_as_float(sw[1]);
    aa += dpp<XOR3>(dpp<XOR7>(aa));
    ldsf[L_QRED + wave * 32 + 8 * cq + ((lane >> 3) & 1) * 4 + ((lane >> 4) & 1) * 2 + (lane >> 5)] = aa;
    asm volatile("" :: "v"(cpf0), "v"(cpf1));
  } else {
    asm volatile("" :: "v"(cpf0), "v"(cpf1));
    const u32 ep = ep_request(p.ws);
    if (wave == NSW) {
      if (lane < 8) lds[L_FLAG + lane] = lane == F_PROG ? (u32)WIN_UNITS * 8u : 0u;
      const u32 q1 = isA ? Q1A : Q1N;
#pragma unroll
      for (int i = 0; i < (UQ_N + 63) / 64; ++i) { const u32 t = (u32)(lane + 64 * i); if (t < UQ_N) lds[L_BQ + t] = t < q1 ? p1_unit(t, s, h) : UQ_EMPTY; }
    } else if (brq == 1 && (col0 & 127) == 0) {
      if (lane == 0) __hip_atomic_store((GAS u32*)(p.ws + WS_CNT + (size_t)(col0 >> 7) * 256), 0u, RLX_AGENT);
      asm volatile("s_waitcnt vmcnt(0)" ::: "memory");
    }
    tag = ep_tag(ep);
  }
  FENCE();
  asm volatile("s_waitcnt lgkmcnt(0)" ::: "memory");
  __builtin_amdgcn_s_barrier();
  FENCE();
  if (wave == NSW + 1) {
    float qs = 0.f;
#pragma unroll
    for (int w = 0; w < NSW; ++w) qs += ldsf[L_QRED + w * 32 + (lane & 31)];
    const u32 pair = pack_bf16(qs, dpp<XOR1>(qs));
    const u32 off = (u32)WS_QG + (u32)((brq * 32 + (col0 >> 7)) * 64 + ((col0 & 127) >> 1) + (lane >> 1)) * 8u;
    __builtin_amdgcn_raw_buffer_store_b64(u32x2{pair, tag}, rWS, (lane < 32 && (lane & 1) == 0) ? off : 0x40000000u, 0, 16);
  }

  if (__builtin_expect(wave >= NSW, 0)) {
  const int br = wave - NSW;
  __builtin_amdgcn_s_setprio(3);
  if (br == 1) {
    GAS u64* src = (GAS u64*)(p.ws + WS_QG) + (size_t)(32 + h) * 64 + lane;
    const u32 gq = (u32)poll_granule<4>(src, tag);
    lds[L_QB + lane] = gq;
    lds_signal_add(lds + L_FLAG + F_QB, lane);
    lds_wait(lds + L_FLAG + F_TAGOK, 1u);
  } else {
    { u32 t2 = tag; PIN1(t2); }
    lds_signal_add(lds + L_FLAG + F_TAGOK, lane);
    if (isA) {
      GAS u64* src = (GAS u64*)(p.ws + WS_QG) + (size_t)(4 * g) * 64 + lane;
      u64 gq[4];
      for (u32 spins = 0;; ++spins) {
        bool ok = true;
#pragma unroll
        for (int j = 0; j < 4; ++j) { gq[j] = __hip_atomic_load(src + j * 64, RLX_AGENT); ok &= (u32)(gq[j] >> 32) == tag; }
        if (__all(ok) || spins > SPIN_CAP) break;
        __builtin_amdgcn_s_sleep(1);
      }
#pragma unroll
      for (int j = 0; j < 4; ++j) lds[L_QA + j * 64 + lane] = (u32)gq[j];
      lds_signal_add(lds + L_FLAG + F_QA, lane);
    }
  }
  const int rq = lane >> 3, cg = lane & 7;
  u32 wp[8][8];
  u32x4 wa[8], wb[8];
  if (br == 1) {
    const rsrc_t cnt = mk_rsrc(p.ws + WS_CNT + (size_t)h * 256, 4);
    const int coff = lane == 0 ? 0 : 0x40000000;
    const int q1 = (int)(isA ? Q1A : Q1N);
    while ((int)__hip_atomic_load(lds + L_FLAG + F_PROG, RLX_WG) < q1 - (1 + CLAIM_LEAD) * GBLK) __builtin_amdgcn_s_sleep(4);
    FENCE();
    u32 cl = (u32)__builtin_amdgcn_raw_ptr_buffer_atomic_add_i32(1, cnt, coff, 0, 0);
    FENCE();
    int k = 0;
    bool bad;
    do {
      const u32 id = (u32)__builtin_amdgcn_readfirstlane((int)cl);
      bad = id >= NBLK2;
      if (lane < GBLK) __hip_atomic_store(lds + L_BQ + q1 + k * GBLK + lane, bad ? NUNIT : R2 + id * GBLK + (u32)lane, RLX_WG);
      ++k;
      if (!bad) {
        while ((int)__hip_atomic_load(lds + L_FLAG + F_PROG, RLX_WG) < q1 + (k - 1 - CLAIM_LEAD) * GBLK) __builtin_amdgcn_s_sleep(1);
        FENCE();
        cl = (u32)__builtin_amdgcn_raw_ptr_buffer_atomic_add_i32(1, cnt, coff, 0, 0);
        FENCE();
      }
    } while (!bad);
    if (lane < 32) __hip_atomic_store(lds + L_BQ + q1 + k * GBLK + lane, NUNIT, RLX_WG);
  }
  {
    const GAS u32x4* w = (const GAS u32x4*)((br ? p.wo[1] : p.wo[0]) + ((size_t)h * DH + 2 * rq) * DM + s * 64 + cg * 8);
#pragma unroll
    for (int i = 0; i < 8; ++i) {
      wa[i] = __builtin_nontemporal_load(w + (size_t)(16 * i) * (DM / 8));
      wb[i] = __builtin_nontemporal_load(w + (size_t)(16 * i + 1) * (DM / 8));
    }
#pragma unroll
    for (int i = 0; i < 8; ++i)
#pragma unroll
      for (int k = 0; k < 4; ++k) {
        wp[i][2 * k] = __builtin_amdgcn_perm(wb[i][k], wa[i][k], 0x05040100u);
        wp[i][2 * k + 1] = __builtin_amdgcn_perm(wb[i][k], wa[i][k], 0x07060302u);
      }
  }
  {
    const bool live = (br == 1) || isA;
    if (live) lds_wait(lds + L_FLAG + (br ? F_CNTB : F_CNTA), (u32)NSW);
    const int nh = (br == 0 && isA) ? 4 : 1;
    for (int j = 0; j < nh; ++j) {
      const LAS float* rec = ldsf + (br ? L_PB : L_PA + j * PREC);
      const int stride = br ? PREC : 4 * PREC;
      float mw[NSW], M = -INFINITY;
#pragma unroll
      for (int w = 0; w < NSW; ++w) { mw[w] = rec[w * stride + 128]; M = fmaxf(M, mw[w]); }
      float L = 0.f, o0 = 0.f, o1 = 0.f;
#pragma unroll
      for (int w = 0; w < NSW; ++w) {
        const float f = ex2((mw[w] - M) * C2);
        const f32x2 o = *(const LAS f32x2*)(rec + w * stride + 2 * lane);
        L += f * rec[w * stride + 129]; o0 += f * o[0]; o1 += f * o[1];
      }
      const u32 slot = br ? (u32)((32 + h) * 8 + s) : (u32)((4 * g + j) * 8 + sA);
      const u32 dead = live ? 0u : 0x40000000u;
      st_sc1(u32x4{__float_as_uint(o0), tag, __float_as_uint(o1), tag}, rWS, (u32)WS_PO + slot * 1024u + (u32)lane * 16u + dead, 0);
      st_sc1(u32x4{__float_as_uint(M * C2), tag, __float_as_uint(L), tag}, rWS, (u32)WS_PML + slot * 16u + (lane == 0 ? dead : 0x40000000u), 0);
    }
  }
  {
    const u32 slot0 = (u32)((br * 32 + h) * 8);
    u32x4 po[8], ml;
    u32 have = 0;
    if (br == 0) {
      for (u32 spins = 0;; ++spins) {
        asm volatile("" ::: "memory");
        ml = ld_sc1(rWS, (u32)WS_PML + (slot0 + (u32)(lane & 7)) * 16u, 0);
        if (__all((ml[1] == tag) & (ml[3] == tag)) || spins > SPIN_CAP) break;
        __builtin_amdgcn_s_sleep(64);
      }
    }
    for (u32 spins = 0;; ++spins) {
      asm volatile("" ::: "memory");
      ml = ld_sc1(rWS, (u32)WS_PML + (slot0 + (u32)(lane & 7)) * 16u, 0);
#pragma unroll
      for (int sp = 0; sp < 8; ++sp)
        if (!((have >> sp) & 1u)) po[sp] = ld_sc1(rWS, (u32)WS_PO + (slot0 + sp) * 1024u + (u32)lane * 16u, 0);
      const u64 mlok = __ballot((ml[1] == tag) & (ml[3] == tag));
#pragma unroll
      for (int sp = 0; sp < 8; ++sp)
        if (!((have >> sp) & 1u)) { if (__all((po[sp][1] == tag) & (po[sp][3] == tag))) have |= 1u << sp; }
      if ((have == 0xffu && (mlok & 0xffu) == 0xffu) || spins > SPIN_CAP) break;
      if (br) __builtin_amdgcn_s_sleep(1); else __builtin_amdgcn_s_sleep(16);
    }
    const float ms = __uint_as_float(ml[0]);
    float M = fmaxf(ms, dpp<XOR1>(ms)); M = fmaxf(M, dpp<XOR2>(M)); M = fmaxf(M, dpp<XOR7>(M));
    const float f = ex2(ms - M);
    float L = f * __uint_as_float(ml[2]); L += dpp<XOR1>(L); L += dpp<XOR2>(L); L += dpp<XOR7>(L);
    float o0 = 0.f, o1 = 0.f;
#pragma unroll
    for (int sp = 0; sp < 8; ++sp) {
      const float fs = __builtin_bit_cast(float, __builtin_amdgcn_readlane(__builtin_bit_cast(int, f), sp));
      o0 += fs * __uint_as_float(po[sp][0]); o1 += fs * __uint_as_float(po[sp][2]);
    }
    const float inv = 1.0f / L;
    lds[L_OB + br * 64 + lane] = pack_bf16(o0 * inv, o1 * inv);
    asm volatile("s_waitcnt lgkmcnt(0)" ::: "memory");
    float y[8];
#pragma unroll
    for (int j = 0; j < 8; ++j) y[j] = 0.f;
#pragma unroll
    for (int i = 0; i < 8; ++i) {
      const u32 ob = lds[L_OB + br * 64 + rq + 8 * i];
#pragma unroll
      for (int j = 0; j < 8; ++j) y[j] = dot2(wp[i][j], ob, y[j]);
    }
#pragma unroll
    for (int j = 0; j < 4; ++j) {
      const float t1 = y[j] + dpp<XOR8>(y[j]), t2 = y[j + 4] + dpp<XOR8>(y[j + 4]);
      y[j] = (lane & 8) ? t2 : t1;
    }
#pragma unroll
    for (int j = 0; j < 2; ++j) {
      auto sw = __builtin_amdgcn_permlane16_swap(__float_as_uint(y[j]), __float_as_uint(y[j + 2]), false, false);
      y[j] = __uint_as_float(sw[0]) + __uint_as_float(sw[1]);
    }
    auto sw = __builtin_amdgcn_permlane32_swap(__float_as_uint(y[0]), __float_as_uint(y[1]), false, false);
    const float yy = __uint_as_float(sw[0]) + __uint_as_float(sw[1]);
    const int col = 8 * cg + ((lane >> 3) & 1) * 4 + ((lane >> 4) & 1) * 2 + (lane >> 5);
    GAS u64* dst = (GAS u64*)(p.ws + WS_OP) + (size_t)(br * 32 + h) * 512 + s * 64 + col;
    __hip_atomic_store(dst, ((u64)tag << 32) | __float_as_uint(yy), RLX_AGENT);
  }
  {
    GAS u64* src = (GAS u64*)(p.ws + WS_OP) + (size_t)(br * 32 + (lane >> 1)) * 512 + 2 * b + (lane & 1);
    u64 gy;
    if (br == 0) {
      for (u32 spins = 0;; ++spins) {
        gy = __hip_atomic_load(src, RLX_AGENT);
        if (__all((u32)(gy >> 32) == tag) || spins > SPIN_CAP) break;
        __builtin_amdgcn_s_sleep(16);
      }
    } else gy = poll_granule<2>(src, tag);
    float y = __uint_as_float((u32)gy);
    y += dpp<XOR2>(y); y += dpp<ROR4>(y); y += dpp<XOR8>(y);
    y = xrow_sum(y);
    const float yn = dpp<XOR1>(y);
    if (lane == 0) *(GAS u32*)(p.out + br * 512 + 2 * b) = pack_bf16(y, yn);
    if (br == 1 && b == 0 && lane == 0) __hip_atomic_store((GAS u32*)(p.ws + WS_EPOCH), tag, RLX_AGENT);
  }
  asm volatile("LK_END2:");
  return;
  }
  {
    asm volatile("s_waitcnt vmcnt(1)" ::: "memory"); FirstLoads<PRE, 8>::go(ring, fK, fV, voff, sof);
    asm volatile("s_waitcnt vmcnt(1)" ::: "memory"); FirstLoads<8, 12>::go(ring, fK, fV, voff, sof);
    asm volatile("s_waitcnt vmcnt(1)" ::: "memory"); FirstLoads<12, 16>::go(ring, fK, fV, voff, sof);
    if (__builtin_expect(!isA, 1)) ExtLoads<16>::go(ring, rKB, rVB, voff, sob);
    if (__builtin_expect(isA, 0)) {
      lds_wait(lds + L_FLAG + F_QA, 1u);
      u32x4 qa[4];
#pragma unroll
      for (int j = 0; j < 4; ++j) qa[j] = *(LAS u32x4*)(lds + L_QA + j * 64 + 4 * c);
      float mA = -INFINITY, lA = 0.f, acc[4][8];
#pragma unroll
      for (int j = 0; j < 4; ++j)
#pragma unroll
        for (int k = 0; k < 8; ++k) acc[j][k] = 0.f;
      a_step<0>(ring, qa, mA, lA, acc, rKA, rVA, rKB, rVB, voff, aoff, sob, c);
      a_step<1>(ring, qa, mA, lA, acc, rKA, rVA, rKB, rVB, voff, aoff, sob, c);
      a_step<2>(ring, qa, mA, lA, acc, rKA, rVA, rKB, rVB, voff, aoff, sob, c);
      a_step<3>(ring, qa, mA, lA, acc, rKA, rVA, rKB, rVB, voff, aoff, sob, c);
      const float M = xrow_max(mA);
      const float f = ex2((mA - M) * C2);
      float L = lA * f; L += dpp<XOR1>(L); L += dpp<XOR2>(L); L = xrow_sum(L);
      const float f0 = dpp<BCAST + 0>(f), f1 = dpp<BCAST + 4>(f), f2 = dpp<BCAST + 8>(f), f3 = dpp<BCAST + 12>(f);
#pragma unroll
      for (int k = 0; k < 8; ++k) {
        acc[0][k] = xrow_sum(acc[0][k] * f0); acc[1][k] = xrow_sum(acc[1][k] * f1);
        acc[2][k] = xrow_sum(acc[2][k] * f2); acc[3][k] = xrow_sum(acc[3][k] * f3);
      }
      if (r == 0) {
#pragma unroll
        for (int j = 0; j < 4; ++j) {
          LAS float* rec = ldsf + L_PA + (wave * 4 + j) * PREC;
          *(LAS f32x4*)(rec + 8 * c) = f32x4{acc[j][0], acc[j][1], acc[j][2], acc[j][3]};
          *(LAS f32x4*)(rec + 8 * c + 4) = f32x4{acc[j][4], acc[j][5], acc[j][6], acc[j][7]};
        }
        if ((c & 3) == 0) { LAS float* rec = ldsf + L_PA + (wave * 4 + (c >> 2)) * PREC; rec[128] = M; rec[129] = L; }
      }
      lds_signal_add(lds + L_FLAG + F_CNTA, lane);
      FENCE();
      FirstLoads<16, RS>::go(ring, rKB, rVB, voff, sob);
      asm volatile("LK_END3:");
    }
    lds_wait(lds + L_FLAG + F_QB, 1u);
    const u32x4 q = *(LAS u32x4*)(lds + L_QB + 4 * c);
    float m = -INFINITY, l = 0.f, acc[8];
#pragma unroll
    for (int k = 0; k < 8; ++k) acc[k] = 0.f;
    bool cur;
    {
      const u32 s0 = next_unit(lds, lane, cur);
      b_step<true>(ring, q, m, l, acc, rKB, rVB, voff, s0, c, true);
#pragma unroll
      for (int u = 1; u < WIN_UNITS; ++u) b_step<false>(ring + u * HS, q, m, l, acc, rKB, rVB, voff, 0, c, true);
    }
    do {
      bool nx;
      const u32 s0 = next_unit(lds, lane, nx);
      b_step<true>(ring, q, m, l, acc, rKB, rVB, voff, s0, c, cur);
      cur = nx;
    } while (cur);
    {
      const float M = xrow_max(m);
      const float f = ex2((m - M) * C2);
      float L = l * f; L += dpp<XOR1>(L); L += dpp<XOR2>(L); L += dpp<XOR7>(L); L += dpp<XOR8>(L); L = xrow_sum(L) * ((float)NV / 16.f);
#pragma unroll
      for (int k = 0; k < 8; ++k) acc[k] = xrow_sum(acc[k] * f);
      if (r == 0) {
        LAS float* rec = ldsf + L_PB + wave * PREC;
        *(LAS f32x4*)(rec + 8 * c) = f32x4{acc[0], acc[1], acc[2], acc[3]};
        *(LAS f32x4*)(rec + 8 * c + 4) = f32x4{acc[4], acc[5], acc[6], acc[7]};
        if (c == 0) { rec[128] = M; rec[129] = L; }
      }
      lds_signal_add(lds + L_FLAG + F_CNTB, lane);
    }
    asm volatile("LK_END1:");
  }
}

}

extern "C" void kernel_launch(void* const* d_in, const int* in_sizes, int n_in, void* d_out, int out_size, void* d_ws,
                              size_t ws_size, hipStream_t stream) {
  Params p;
  p.x[0] = (const uint16_t*)d_in[0]; p.k[0] = (const uint16_t*)d_in[1]; p.v[0] = (const uint16_t*)d_in[2];
  p.wq[0] = (const uint16_t*)d_in[3]; p.wo[0] = (const uint16_t*)d_in[4];
  p.x[1] = (const uint16_t*)d_in[5]; p.k[1] = (const uint16_t*)d_in[6]; p.v[1] = (const uint16_t*)d_in[7];
  p.wq[1] = (const uint16_t*)d_in[8]; p.wo[1] = (const uint16_t*)d_in[9];
  p.out = (uint16_t*)d_out; p.ws = (unsigned char*)d_ws;
  hipLaunchKernelGGL(fused_decode_kernel, dim3(NWG), dim3(NTHR), 0, stream, p);
}
```
